```python
import torch

def _softmax(x, axis=-1):
    return torch.softmax(x, dim=axis)
def _gelu(x):
    return torch.nn.functional.gelu(x, approximate="tanh")
def _silu(x):
    return torch.nn.functional.silu(x)
def _sigmoid(x):
    return torch.sigmoid(x)
def _layernorm(x, axis=-1, eps=1e-5):
    m = x.mean(dim=axis, keepdim=True)
    v = x.var(dim=axis, keepdim=True, unbiased=False)
    return (x - m) / torch.sqrt(v + eps)
def _scatter_add(tgt, idx, src, axis=0):
    return tgt.clone().index_add_(axis, idx, src)
def _segment_sum(src, seg, n):
    out = torch.zeros((n,) + src.shape[1:], dtype=src.dtype)
    return out.index_add_(0, seg, src)
def _pad(x, pw):
    flat = tuple(v for pair in reversed(pw) for v in pair)
    return torch.nn.functional.pad(x, flat)

kernel_name = 'gated_mix__ssm_conv_s5__linear_kernel_hedgehog__prompt'

def setup_inputs():

    _w = (lambda a: (lambda t: t.to(torch.bfloat16) if t.is_floating_point() else t)(torch.as_tensor(a)))
    def _A_setup():
        B, S, D = 1, 2048, 576
        P = D
        Lambda_re = -0.5 * (lambda _x: torch.exp(torch.as_tensor(_x)))((lambda *s: torch.randn(*s, dtype=torch.bfloat16) * 0.8)(P))
        Lambda_im = (lambda *s: torch.randn(*s, dtype=torch.bfloat16) * 0.8)(P) * 1.0
        B_re = (lambda *s: torch.randn(*s, dtype=torch.bfloat16) * 0.8)(P, D) * (D ** -0.5)
        B_im = (lambda *s: torch.randn(*s, dtype=torch.bfloat16) * 0.8)(P, D) * (D ** -0.5)
        C_re = (lambda *s: torch.randn(*s, dtype=torch.bfloat16) * 0.8)(D, P) * (P ** -0.5)
        C_im = (lambda *s: torch.randn(*s, dtype=torch.bfloat16) * 0.8)(D, P) * (P ** -0.5)
        log_dt = (lambda *s: torch.randn(*s, dtype=torch.bfloat16) * 0.8)(P) * 0.3 - 2.0
        Dskip = (lambda *s: torch.randn(*s, dtype=torch.bfloat16) * 0.8)(D)
        return {
            "x": _w((lambda *s: torch.randn(*s, dtype=torch.bfloat16) * 0.05)(B, S, D)),
            "Lambda_re": _w(Lambda_re),
            "Lambda_im": _w(Lambda_im),
            "B_re": _w(B_re),
            "B_im": _w(B_im),
            "C_re": _w(C_re),
            "C_im": _w(C_im),
            "log_dt": _w(log_dt),
            "Dskip": _w(Dskip),
        }
    _A_in = _A_setup()

    def _B_setup():
        B, S, D, H = 1, 2048, 576, 32
        Dh = D // H
        return {
            "x": _w((lambda *s: torch.randn(*s, dtype=torch.bfloat16) * 0.05)(B, S, D)),
            "wqkv": _w((lambda *s: torch.randn(*s, dtype=torch.bfloat16) * 1.6)(D, 3 * D)),
            "w_phi": _w((lambda *s: torch.randn(*s, dtype=torch.bfloat16) * 1.6)(H, Dh, Dh) * (Dh ** -0.5)),
            "wo": _w((lambda *s: torch.randn(*s, dtype=torch.bfloat16) * 0.05)(D, D)),
        }
    _B_in = _B_setup()

    return {
        "A_x": _A_in["x"],
        "A_Lambda_re": _A_in["Lambda_re"],
        "A_Lambda_im": _A_in["Lambda_im"],
        "A_B_re": _A_in["B_re"],
        "A_B_im": _A_in["B_im"],
        "A_C_re": _A_in["C_re"],
        "A_C_im": _A_in["C_im"],
        "A_log_dt": _A_in["log_dt"],
        "A_Dskip": _A_in["Dskip"],
        "B_x": _B_in["x"],
        "B_wqkv": _B_in["wqkv"],
        "B_w_phi": _B_in["w_phi"],
        "B_wo": _B_in["wo"],
        "gate_w": _w((lambda *s: torch.randn(*s, dtype=torch.bfloat16) * 0.05)(576) * 16.0),
    }

def reference(A_x, A_Lambda_re, A_Lambda_im, A_B_re, A_B_im, A_C_re, A_C_im, A_log_dt, A_Dskip, B_x, B_wqkv, B_w_phi, B_wo, gate_w):
    def _A_ref(x, Lambda_re, Lambda_im, B_re, B_im, C_re, C_im, log_dt, Dskip):
        Bsz, L, H = x.shape
        Lam = Lambda_re + 1j * Lambda_im
        Bmat = B_re + 1j * B_im
        Cmat = C_re + 1j * C_im
        dt = torch.exp(log_dt)
        Lam_bar = torch.exp(dt * Lam)
        B_bar = ((Lam_bar - 1.0) / Lam)[:, None] * Bmat
        Bu = torch.einsum("bld,pd->blp", x + 0j, B_bar)
        h = torch.zeros_like(Bu[:, 0, :])
        hs = []
        for t in range(L):
            h = Lam_bar[None, :] * h + Bu[:, t, :]
            hs.append(h)
        hseq = torch.stack(hs, 1)
        y = 2.0 * torch.einsum("blp,dp->bld", hseq, Cmat).real + x * Dskip[None, None, :]
        return y

    def _B_ref(x, wqkv, w_phi, wo):
        B, S, D = x.shape
        H = 32; Dh = D // H
        qkv = (lambda x, *d: x.permute(*d))((x @ wqkv).reshape(B, S, 3, H, Dh), 2, 0, 3, 1, 4)
        q, k, v = qkv[0], qkv[1], qkv[2]
        pq = torch.einsum("bhsd,hdf->bhsf", q, w_phi)
        pk = torch.einsum("bhsd,hdf->bhsf", k, w_phi)
        phi_q = torch.concatenate([_softmax(pq, axis=-1), _softmax(-pq, axis=-1)], axis=-1)
        phi_k = torch.concatenate([_softmax(pk, axis=-1), _softmax(-pk, axis=-1)], axis=-1)
        kv = torch.einsum("bhsf,bhsd->bhfd", phi_k, v)
        z  = torch.einsum("bhsf->bhf", phi_k)
        num = torch.einsum("bhqf,bhfd->bhqd", phi_q, kv)
        den = torch.einsum("bhqf,bhf->bhq", phi_q, z)[..., None] + 1e-6
        o = num / den
        return (lambda x, *d: x.permute(*d))(o, 0, 2, 1, 3).reshape(B, S, D) @ wo

    x = A_x
    g = _sigmoid((x * gate_w).sum(-1, keepdims=True))
    a = _A_ref(x, A_Lambda_re, A_Lambda_im, A_B_re, A_B_im, A_C_re, A_C_im, A_log_dt, A_Dskip)
    b = _B_ref(x, B_wqkv, B_w_phi, B_wo)
    return g * a + (1 - g) * b
```

```cpp
#include <hip/hip_runtime.h>
#include <hip/hip_bf16.h>
#include <stdint.h>

typedef unsigned short u16;
__device__ __forceinline__ float bf2f(u16 v) { return __uint_as_float(((unsigned)v) << 16); }
__device__ __forceinline__ float rbf(float f) { return __bfloat162float(__float2bfloat16(f)); }

#define L_ 2048
#define D_ 576
#define H_ 32
#define DH 18

__global__ void k_gemm1(const u16* x, const u16* Bre, const u16* Bim, const u16* wqkv, float* bu, float* qkv) {
    int n = blockIdx.x * 64 + threadIdx.x;
    int t = blockIdx.y;
    if (n >= 2880) return;
    float acc = 0.f;
    if (n < 1152) {
        const u16* w = (n < 576) ? Bre + n * D_ : Bim + (n - 576) * D_;
        for (int k = 0; k < D_; k++) acc += bf2f(x[t * D_ + k]) * bf2f(w[k]);
        bu[t * 1152 + n] = acc;
    } else {
        int c = n - 1152;
        for (int k = 0; k < D_; k++) acc += bf2f(x[t * D_ + k]) * bf2f(wqkv[k * 1728 + c]);
        qkv[t * 1728 + c] = acc;
    }
}

__global__ void k_scan(const u16* lre, const u16* lim, const u16* logdt, float* bu) {
    int p = blockIdx.x * 64 + threadIdx.x;
    if (p >= 576) return;
    float dt = rbf(expf(bf2f(logdt[p])));
    float a = bf2f(lre[p]), b = bf2f(lim[p]);
    float e = expf(dt * a);
    float lr = e * cosf(dt * b), li = e * sinf(dt * b);
    float nr = lr - 1.f, ni = li;
    float den = a * a + b * b;
    float cr = (nr * a + ni * b) / den, ci = (ni * a - nr * b) / den;
    float hr = 0, hi = 0;
    for (int t = 0; t < L_; t++) {
        float ur = bu[t * 1152 + p], ui = bu[t * 1152 + 576 + p];
        float vr = cr * ur - ci * ui, vi = cr * ui + ci * ur;
        float nhr = lr * hr - li * hi + vr;
        float nhi = lr * hi + li * hr + vi;
        hr = nhr; hi = nhi;
        bu[t * 1152 + p] = hr; bu[t * 1152 + 576 + p] = hi;
    }
}

__global__ void k_phi(const float* qkv, const u16* wphi, float* phiq, float* phik) {
    int idx = blockIdx.x * 64 + threadIdx.x;
    int t = idx / 64, which = (idx % 64) / 32, h = idx % 32;
    float q[DH], p[DH];
#pragma unroll
    for (int d = 0; d < DH; d++) q[d] = qkv[t * 1728 + which * 576 + h * DH + d];
#pragma unroll
    for (int f = 0; f < DH; f++) { float a = 0;
#pragma unroll
        for (int d = 0; d < DH; d++) a += q[d] * bf2f(wphi[h * 324 + d * DH + f]);
        p[f] = a; }
    float mx = p[0], mn = p[0];
#pragma unroll
    for (int f = 1; f < DH; f++) { mx = fmaxf(mx, p[f]); mn = fminf(mn, p[f]); }
    float s1 = 0, s2 = 0;
#pragma unroll
    for (int f = 0; f < DH; f++) { s1 += expf(p[f] - mx); s2 += expf(mn - p[f]); }
    float* o = (which ? phik : phiq) + t * 1152 + h * 36;
#pragma unroll
    for (int f = 0; f < DH; f++) { o[f] = expf(p[f] - mx) / s1; o[18 + f] = expf(mn - p[f]) / s2; }
}

__global__ void k_kv(const float* qkv, const float* phik, float* kv) {
    int idx = blockIdx.x * 64 + threadIdx.x;
    if (idx >= 32 * 36 * 19) return;
    int h = idx / (36 * 19), f = (idx / 19) % 36, d = idx % 19;
    float a = 0;
    for (int t = 0; t < L_; t++) {
        float v = d < 18 ? qkv[t * 1728 + 1152 + h * DH + d] : 1.f;
        a += phik[t * 1152 + h * 36 + f] * v;
    }
    kv[idx] = a;
}

__global__ void k_o(const float* phiq, const float* kv, float* o) {
    int idx = blockIdx.x * 64 + threadIdx.x;
    int t = idx / 576, c = idx % 576, h = c / DH, d = c % DH;
    float num = 0, den = 0;
    for (int f = 0; f < 36; f++) {
        float p = phiq[t * 1152 + h * 36 + f];
        num += p * kv[h * 36 * 19 + f * 19 + d];
        den += p * kv[h * 36 * 19 + f * 19 + 18];
    }
    o[idx] = num / (den + 1e-6f);
}

__global__ void k_out(const u16* x, const float* hs, const float* o, const u16* Cre, const u16* Cim, const u16* wo,
                      const u16* dskip, const u16* gw, float* out) {
    int n = blockIdx.x * 64 + threadIdx.x; int t = blockIdx.y;
    float gs = 0;
    for (int k = 0; k < D_; k++) gs += bf2f(x[t * D_ + k]) * bf2f(gw[k]);
    float g = 1.f / (1.f + expf(-gs));
    float a = 0, b = 0;
    for (int p = 0; p < 576; p++) a += hs[t * 1152 + p] * bf2f(Cre[n * 576 + p]) - hs[t * 1152 + 576 + p] * bf2f(Cim[n * 576 + p]);
    for (int k = 0; k < 576; k++) b += o[t * 576 + k] * bf2f(wo[k * 576 + n]);
    a = 2.f * a + bf2f(x[t * D_ + n]) * bf2f(dskip[n]);
    out[t * D_ + n] = g * a + (1.f - g) * b;
}

extern "C" void kernel_launch(void* const* d_in, const int* in_sizes, int n_in,
                              void* d_out, int out_size, void* d_ws, size_t ws_size,
                              hipStream_t stream) {
    const u16* x = (const u16*)d_in[0];
    const u16* lre = (const u16*)d_in[1];
    const u16* lim = (const u16*)d_in[2];
    const u16* Bre = (const u16*)d_in[3];
    const u16* Bim = (const u16*)d_in[4];
    const u16* Cre = (const u16*)d_in[5];
    const u16* Cim = (const u16*)d_in[6];
    const u16* logdt = (const u16*)d_in[7];
    const u16* dskip = (const u16*)d_in[8];
    const u16* wqkv = (const u16*)d_in[10];
    const u16* wphi = (const u16*)d_in[11];
    const u16* wo = (const u16*)d_in[12];
    const u16* gw = (const u16*)d_in[13];
    float* ws = (float*)d_ws;
    float* bu = ws; ws += L_ * 1152;
    float* qkv = ws; ws += L_ * 1728;
    float* phiq = ws; ws += L_ * 1152;
    float* phik = ws; ws += L_ * 1152;
    float* kv = ws; ws += 32 * 36 * 19 + 64;
    float* o = ws; ws += L_ * 576;
    k_gemm1<<<dim3(45, L_), 64, 0, stream>>>(x, Bre, Bim, wqkv, bu, qkv);
    k_scan<<<9, 64, 0, stream>>>(lre, lim, logdt, bu);
    k_phi<<<L_, 64, 0, stream>>>(qkv, wphi, phiq, phik);
    k_kv<<<(32 * 36 * 19 + 63) / 64, 64, 0, stream>>>(qkv, phik, kv);
    k_o<<<L_ * 576 / 64, 64, 0, stream>>>(phiq, kv, o);
    k_out<<<dim3(9, L_), 64, 0, stream>>>(x, bu, o, Cre, Cim, wo, dskip, gw, (float*)d_out);
}
```

```cpp
#define MAPV 0
#define WHAT 2
#include <hip/hip_runtime.h>
#include <hip/hip_bf16.h>
#include <stdint.h>

typedef unsigned short u16;
__device__ __forceinline__ float bf2f(u16 v) { return __uint_as_float(((unsigned)v) << 16); }
__device__ __forceinline__ float rbf(float f) { return __bfloat162float(__float2bfloat16(f)); }

#define L_ 2048
#define D_ 576
#define H_ 32
#define DH 18

__global__ void k_gemm1(const u16* x, const u16* Bre, const u16* Bim, const u16* wqkv, float* bu, float* qkv) {
    int n = blockIdx.x * 64 + threadIdx.x;
    int t = blockIdx.y;
    if (n >= 2880) return;
    float acc = 0.f;
    if (n < 1152) {
        const u16* w = (n < 576) ? Bre + n * D_ : Bim + (n - 576) * D_;
        for (int k = 0; k < D_; k++) acc += bf2f(x[t * D_ + k]) * bf2f(w[k]);
        bu[t * 1152 + n] = acc;
    } else {
        int c = n - 1152;
        for (int k = 0; k < D_; k++) acc += bf2f(x[t * D_ + k]) * bf2f(wqkv[k * 1728 + c]);
        qkv[t * 1728 + c] = acc;
    }
}

__global__ void k_scan(const u16* lre, const u16* lim, const u16* logdt, float* bu) {
    int p = blockIdx.x * 64 + threadIdx.x;
    if (p >= 576) return;
    float dt = rbf(expf(bf2f(logdt[p])));
    float a = bf2f(lre[p]), b = bf2f(lim[p]);
    float e = expf(dt * a);
    float lr = e * cosf(dt * b), li = e * sinf(dt * b);
    float nr = lr - 1.f, ni = li;
    float den = a * a + b * b;
    float cr = (nr * a + ni * b) / den, ci = (ni * a - nr * b) / den;
    float hr = 0, hi = 0;
    for (int t = 0; t < L_; t++) {
        float ur = bu[t * 1152 + p], ui = bu[t * 1152 + 576 + p];
        float vr = cr * ur - ci * ui, vi = cr * ui + ci * ur;
        float nhr = lr * hr - li * hi + vr;
        float nhi = lr * hi + li * hr + vi;
        hr = nhr; hi = nhi;
        bu[t * 1152 + p] = hr; bu[t * 1152 + 576 + p] = hi;
    }
}

__global__ void k_phi(const float* qkv, const u16* wphi, float* phiq, float* phik) {
    int idx = blockIdx.x * 64 + threadIdx.x;
    int t = idx / 64, which = (idx % 64) / 32, h = idx % 32;
    float q[DH], p[DH];
#pragma unroll
    for (int d = 0; d < DH; d++) q[d] = qkv[t * 1728 + which * 576 + h * DH + d];
#pragma unroll
    for (int f = 0; f < DH; f++) { float a = 0;
#pragma unroll
        for (int d = 0; d < DH; d++) a += q[d] * bf2f(wphi[h * 324 + d * DH + f]);
        p[f] = a; }
    float mx = p[0], mn = p[0];
#pragma unroll
    for (int f = 1; f < DH; f++) { mx = fmaxf(mx, p[f]); mn = fminf(mn, p[f]); }
    float s1 = 0, s2 = 0;
#pragma unroll
    for (int f = 0; f < DH; f++) { s1 += expf(p[f] - mx); s2 += expf(mn - p[f]); }
    float* o = (which ? phik : phiq) + t * 1152 + h * 36;
#pragma unroll
    for (int f = 0; f < DH; f++) { o[f] = expf(p[f] - mx) / s1; o[18 + f] = expf(mn - p[f]) / s2; }
}

__global__ void k_kv(const float* qkv, const float* phik, float* kv) {
    int idx = blockIdx.x * 64 + threadIdx.x;
    if (idx >= 32 * 36 * 19) return;
    int h = idx / (36 * 19), f = (idx / 19) % 36, d = idx % 19;
    float a = 0;
    for (int t = 0; t < L_; t++) {
        float v = d < 18 ? qkv[t * 1728 + 1152 + h * DH + d] : 1.f;
        a += phik[t * 1152 + h * 36 + f] * v;
    }
    kv[idx] = a;
}

__global__ void k_o(const float* phiq, const float* kv, float* o) {
    int idx = blockIdx.x * 64 + threadIdx.x;
    int t = idx / 576, c = idx % 576, h = c / DH, d = c % DH;
    float num = 0, den = 0;
    for (int f = 0; f < 36; f++) {
        float p = phiq[t * 1152 + h * 36 + f];
        num += p * kv[h * 36 * 19 + f * 19 + d];
        den += p * kv[h * 36 * 19 + f * 19 + 18];
    }
    o[idx] = num / (den + 1e-6f);
}

__global__ void k_out(const u16* x, const float* hs, const float* o, const u16* Cre, const u16* Cim, const u16* wo,
                      const u16* dskip, const u16* gw, float* out) {
    int n = blockIdx.x * 64 + threadIdx.x; int t = blockIdx.y;
    float gs = 0;
    for (int k = 0; k < D_; k++) gs += bf2f(x[t * D_ + k]) * bf2f(gw[k]);
    float g = 1.f / (1.f + expf(-gs));
    float a = 0, b = 0;
    for (int p = 0; p < 576; p++) a += hs[t * 1152 + p] * bf2f(Cre[n * 576 + p]) - hs[t * 1152 + 576 + p] * bf2f(Cim[n * 576 + p]);
    for (int k = 0; k < 576; k++) b += o[t * 576 + k] * bf2f(wo[k * 576 + n]);
    a = 2.f * a + bf2f(x[t * D_ + n]) * bf2f(dskip[n]);
    out[t * D_ + n] = g * a + (1.f - g) * b;
}

typedef unsigned long long u64;
#define INFL 10
#define STR2(x) #x
#define STR(x) STR2(x)
__global__ __launch_bounds__(512) void k_probe(const char* x, const char* Bre, const char* Bim, const char* wqkv, u64* stamps, unsigned* sink) {
    extern __shared__ __attribute__((aligned(16))) char smem[];
    u64 t0 = __builtin_amdgcn_s_memrealtime();
    int wid = __builtin_amdgcn_readfirstlane(threadIdx.x >> 6), lane = threadIdx.x & 63;
    int b = blockIdx.x; int xcd = b & 7, r = b >> 3;
    int ti, cj;
    if (MAPV == 0) { ti = xcd * 2 + (r >> 4); cj = r & 15; }
    else if (MAPV == 1) { ti = (xcd & 1) * 8 + (r >> 2); cj = (xcd >> 1) * 4 + (r & 3); }
    else if (MAPV == 2) { ti = (xcd & 3) * 4 + (r >> 3); cj = (xcd >> 2) * 8 + (r & 7); }
    else { ti = r >> 1; cj = xcd * 2 + (r & 1); }
    const char* p[5]; int st[5];
#pragma unroll
    for (int s = 0; s < 5; s++) {
        int g = s * 8 + wid;
        if (g < 16) { p[s] = x + (long)(ti * 128 + g * 8 + (lane >> 3)) * 1152 + (lane & 7) * 16; st[s] = 128; }
        else if (g < 25) { int row = (g - 16) * 8 + (lane >> 3); const char* bb = row < 36 ? Bre : Bim; int rr = row < 36 ? row : row - 36;
            p[s] = bb + (long)(cj * 36 + rr) * 1152 + (lane & 7) * 16; st[s] = 128; }
        else { int e = (g - 25) * 64 + lane; int row = e / 15, q = e % 15; int part = q / 5, c = q % 5;
            int off = (cj * 72) & ~15; if (off + 80 > 1152 && part == 2) off = 1152 - 80;
            p[s] = wqkv + (long)row * 3456 + part * 1152 + off + c * 16; st[s] = 64 * 3456; }
    }
    u64 t1 = 0;
#pragma unroll
    for (int kc = 0; kc < 9; kc++) {
#pragma unroll
        for (int s = 0; s < 5; s++) {
            __builtin_amdgcn_global_load_lds((const __attribute__((address_space(1))) void*)(p[s] + (long)kc * st[s]),
                (__attribute__((address_space(3))) void*)(smem + ((kc % 3) * 40 + s * 8 + wid) * 1024), 16, 0, 0);
        }
        asm volatile("s_waitcnt vmcnt(" STR(INFL) ")" ::: "memory");
        if (kc == 2 && WHAT == 1) { t1 = __builtin_amdgcn_s_memrealtime(); }
    }
    asm volatile("s_waitcnt vmcnt(0)" ::: "memory");
    __syncthreads();
    if (WHAT == 0) t1 = __builtin_amdgcn_s_memrealtime();
    if (WHAT == 2) t1 = t0;
    if (*(unsigned*)(smem + threadIdx.x * 4) == 0x12345678) sink[threadIdx.x] = 1;
    if (threadIdx.x == 0) { stamps[b * 2] = t0; stamps[b * 2 + 1] = t1; }
}
__global__ void k_spin(const u64* stamps) {
    u64 a = stamps[threadIdx.x * 2], e = stamps[threadIdx.x * 2 + 1];
    __shared__ u64 sa[256], se[256];
    sa[threadIdx.x] = a; se[threadIdx.x] = e; __syncthreads();
    if (threadIdx.x == 0) {
        u64 mn = ~0ull, mx = 0;
        for (int i = 0; i < 256; i++) { mn = sa[i] < mn ? sa[i] : mn; mx = se[i] > mx ? se[i] : mx; }
        u64 span = mx - mn; if (span > 5000) span = 5000;
        u64 t = __builtin_amdgcn_s_memrealtime() + span * 1000;
        while (__builtin_amdgcn_s_memrealtime() < t) __builtin_amdgcn_s_sleep(32);
    }
}
extern "C" void kernel_launch(void* const* d_in, const int* in_sizes, int n_in,
                              void* d_out, int out_size, void* d_ws, size_t ws_size,
                              hipStream_t stream) {
    const u16* x = (const u16*)d_in[0];
    const u16* lre = (const u16*)d_in[1];
    const u16* lim = (const u16*)d_in[2];
    const u16* Bre = (const u16*)d_in[3];
    const u16* Bim = (const u16*)d_in[4];
    const u16* Cre = (const u16*)d_in[5];
    const u16* Cim = (const u16*)d_in[6];
    const u16* logdt = (const u16*)d_in[7];
    const u16* dskip = (const u16*)d_in[8];
    const u16* wqkv = (const u16*)d_in[10];
    const u16* wphi = (const u16*)d_in[11];
    const u16* wo = (const u16*)d_in[12];
    const u16* gw = (const u16*)d_in[13];
    float* ws = (float*)d_ws;
    float* bu = ws; ws += L_ * 1152;
    float* qkv = ws; ws += L_ * 1728;
    float* phiq = ws; ws += L_ * 1152;
    float* phik = ws; ws += L_ * 1152;
    float* kv = ws; ws += 32 * 36 * 19 + 64;
    float* o = ws; ws += L_ * 576;
    u64* stamps = (u64*)((char*)d_ws + (200u << 20));
    hipFuncSetAttribute((const void*)k_probe, hipFuncAttributeMaxDynamicSharedMemorySize, 120 * 1024);
    k_probe<<<256, 512, 120 * 1024, stream>>>((const char*)x, (const char*)Bre, (const char*)Bim, (const char*)wqkv, stamps, (unsigned*)(stamps + 1024));
    k_gemm1<<<dim3(45, L_), 64, 0, stream>>>(x, Bre, Bim, wqkv, bu, qkv);
    k_scan<<<9, 64, 0, stream>>>(lre, lim, logdt, bu);
    k_phi<<<L_, 64, 0, stream>>>(qkv, wphi, phiq, phik);
    k_kv<<<(32 * 36 * 19 + 63) / 64, 64, 0, stream>>>(qkv, phik, kv);
    k_o<<<L_ * 576 / 64, 64, 0, stream>>>(phiq, kv, o);
    k_out<<<dim3(9, L_), 64, 0, stream>>>(x, bu, o, Cre, Cim, wo, dskip, gw, (float*)d_out);
    k_spin<<<1, 256, 0, stream>>>(stamps);
}
```

```cpp
#include <hip/hip_runtime.h>
#include <stdint.h>

typedef unsigned short u16;
typedef unsigned long long u64;
typedef short s16x4 __attribute__((ext_vector_type(4)));
typedef __bf16 bf16x2 __attribute__((ext_vector_type(2)));
typedef __bf16 bf16x4 __attribute__((ext_vector_type(4)));
typedef __bf16 bf16x8 __attribute__((ext_vector_type(8)));
typedef float f32x2 __attribute__((ext_vector_type(2)));
typedef float f32x4 __attribute__((ext_vector_type(4)));
typedef unsigned u32x2 __attribute__((ext_vector_type(2)));
typedef unsigned u32x4 __attribute__((ext_vector_type(4)));
#define LAS __attribute__((address_space(3)))
#define GAS __attribute__((address_space(1)))
#define DI __device__ __forceinline__
typedef __amdgpu_buffer_rsrc_t rsrc_t;

#define DBG 0

constexpr unsigned WS_EPOCH = 0;
constexpr unsigned WS_START = 4096;
constexpr unsigned WS_HFLAG = 8192;
constexpr unsigned WS_OFLAG = 24576;
constexpr unsigned WS_G     = 40960;
constexpr unsigned WS_E     = 65536;
constexpr unsigned WS_KV    = 262144;
constexpr unsigned WS_OL    = 1114112;
constexpr unsigned WS_O     = 1376256;
constexpr unsigned WS_H     = WS_O + 128 * 16 * 1024;
constexpr unsigned WS_YA    = WS_H + 128 * 36 * 1024;
constexpr unsigned WS_END   = WS_YA + 128 * 40 * 1024;
constexpr unsigned WS_DBG   = 32u << 20;

constexpr int SPIN_CAP = 1 << 14;
constexpr int AUX_SC1 = 16;

struct Params {
    const char *x, *lre, *lim, *bre, *bim, *cre, *cim, *logdt, *dskip, *wqkv, *wphi, *wo, *gw;
    float* out;
    char* ws;
};

DI float bf2f(unsigned v) { return __uint_as_float(v << 16); }
DI float bflo(unsigned v) { return __uint_as_float(v << 16); }
DI float bfhi(unsigned v) { return __uint_as_float(v & 0xffff0000u); }
DI unsigned pk(float a, float b) { f32x2 t = {a, b}; return __builtin_bit_cast(unsigned, __builtin_convertvector(t, bf16x2)); }
DI f32x4 mfma32(u32x4 a, u32x4 b, f32x4 c) {
    return __builtin_amdgcn_mfma_f32_16x16x32_bf16(__builtin_bit_cast(bf16x8, a), __builtin_bit_cast(bf16x8, b), c, 0, 0, 0);
}
DI f32x4 mfma16(u32x2 a, u32x2 b, f32x4 c) {
    return mfma32(u32x4{a[0], a[1], 0u, 0u}, u32x4{b[0], b[1], 0u, 0u}, c);
}
DI u32x4 lds128(unsigned a) { return *(LAS u32x4*)(size_t)a; }
DI u32x2 lds64(unsigned a) { return *(LAS u32x2*)(size_t)a; }
DI void sts64(unsigned a, u32x2 v) { *(LAS u32x2*)(size_t)a = v; }
DI void sts128(unsigned a, u32x4 v) { *(LAS u32x4*)(size_t)a = v; }
DI u32x2 ldstr(unsigned a) {
    return __builtin_bit_cast(u32x2, __builtin_amdgcn_ds_read_tr16_b64_v4bf16((LAS bf16x4*)(size_t)a));
}
DI u32x4 ldstr2(unsigned a, unsigned d) { u32x2 lo = ldstr(a), hi = ldstr(a + d); return u32x4{lo[0], lo[1], hi[0], hi[1]}; }
DI void dma16(const char* sbase, unsigned voff, unsigned lds) {
    asm volatile("s_mov_b32 m0, %2\n\ts_nop 0\n\tglobal_load_lds_dwordx4 %0, %1" :: "v"(voff), "s"(sbase), "s"(lds) : "memory");
}
template <int N> DI void wait_vm() { asm volatile("s_waitcnt vmcnt(%0)" :: "i"(N) : "memory"); }
DI void wait_lgkm() { asm volatile("s_waitcnt lgkmcnt(0)" ::: "memory"); }
DI void barrier() { asm volatile("s_waitcnt lgkmcnt(0)\n\ts_barrier" ::: "memory"); }
DI int uni(int v) { return __builtin_amdgcn_readfirstlane(v); }
DI u32x4 ld128(rsrc_t r, unsigned v, unsigned s) { return __builtin_amdgcn_raw_buffer_load_b128(r, v, s, AUX_SC1); }
DI u32x2 ld64(rsrc_t r, unsigned v, unsigned s) { return __builtin_amdgcn_raw_buffer_load_b64(r, v, s, AUX_SC1); }
DI void st128(rsrc_t r, unsigned v, unsigned s, u32x4 d) { __builtin_amdgcn_raw_buffer_store_b128(d, r, v, s, AUX_SC1); }
DI void st64(rsrc_t r, unsigned v, unsigned s, u32x2 d) { __builtin_amdgcn_raw_buffer_store_b64(d, r, v, s, AUX_SC1); }
DI void st32(rsrc_t r, unsigned v, unsigned s, unsigned d) { __builtin_amdgcn_raw_buffer_store_b32(d, r, v, s, AUX_SC1); }
DI f32x2 cmul(f32x2 a, f32x2 b) { return f32x2{a[0] * b[0] - a[1] * b[1], a[0] * b[1] + a[1] * b[0]}; }
DI f32x2 cfma(f32x2 a, f32x2 b, f32x2 c) {
    return f32x2{fmaf(a[0], b[0], fmaf(-a[1], b[1], c[0])), fmaf(a[0], b[1], fmaf(a[1], b[0], c[1]))};
}

#define STAMP(i) do {} while (0)
#define CSTART
#define CEND
#define NO_DMA 0
#define NO_CMP 0

DI void poll_flags(rsrc_t ws, unsigned fo, bool need, u32x2 tag) {
#pragma unroll 1
    for (int spin = 0; spin < SPIN_CAP; spin++) {
        asm volatile("" ::: "memory");
        u32x2 f = ld64(ws, fo, 0);
        bool ok = !need || (f[0] == tag[0] && f[1] == tag[1]);
        if (__all(ok)) break;
        __builtin_amdgcn_s_sleep(1);
    }
    asm volatile("" ::: "memory");
}

DI void dma_c(const Params& P, int m, int g0, int Cc, unsigned vrow, unsigned lds) {
    int gi = m >= 18 ? 1 : 0; int mm = m - 18 * gi; int comp = mm >= 9 ? 1 : 0; int n = mm - 9 * comp;
    const char* base = (comp ? P.cim : P.cre) + (size_t)((72 * Cc + 8 * n) * 1152 + (g0 + gi) * 128);
    dma16(base, vrow ^ ((n & 1) << 6), lds + m * 1024);
}

DI void wait_h(rsrc_t ws, int R, int g0, int ng, int lane, u32x2 tag) {
    int t0 = (128 * R) / 160, t1 = (128 * R + 127) / 160;
    int tt = (lane >= 16) ? t1 : t0, g = lane & 15;
    bool need = lane < 32 && g < ng;
    unsigned fo = need ? WS_HFLAG + (unsigned)(tt * 9 + g0 + g) * 128u : WS_HFLAG;
    poll_flags(ws, fo, need, tag);
}

template <int NK>
DI void p2_ssm(rsrc_t ws, f32x4 (&acc)[2][5], int rt0, int k0, int rot, int gfirst, unsigned cbase, int lane) {
    u32x4 hf[NK][2];
#pragma unroll
    for (int i = 0; i < NK; i++) {
        int kk = i + rot; if (kk >= NK) kk -= NK;
        int ks = gfirst * 4 + k0 + kk;
#pragma unroll
        for (int m = 0; m < 2; m++) hf[i][m] = ld128(ws, lane * 16, WS_H + (unsigned)((rt0 + m) * 36 + ks) * 1024u);
    }
    int rho = lane & 15, kq = lane >> 4;
    unsigned lb = (unsigned)((kq >> 1) * 9216 + rho * 128 + (((kq & 1) ^ ((rho >> 1) & 7)) << 4));
#pragma unroll
    for (int i = 0; i < NK; i++) {
        int kk = i + rot; if (kk >= NK) kk -= NK;
        int kl = k0 + kk;
        unsigned a = (lb ^ (unsigned)((kl & 3) << 5)) + cbase + (unsigned)((kl >> 2) * 18432);
#pragma unroll
        for (int t = 0; t < 5; t++) {
            u32x4 w = lds128(a + t * 2048);
#pragma unroll
            for (int m = 0; m < 2; m++) acc[m][t] = mfma32(w, hf[i][m], acc[m][t]);
        }
    }
}

DI void khalf_reduce(f32x4 (&acc)[2][5], f32x4 (&res)[5], unsigned xb, int w, int lane) {
    int kh = w >> 2;
#pragma unroll
    for (int t = 0; t < 5; t++) {
        f32x4 s = kh ? acc[0][t] : acc[1][t];
        sts128(xb + (unsigned)((w * 5 + t) * 1024 + lane * 16), __builtin_bit_cast(u32x4, s));
    }
    barrier();
#pragma unroll
    for (int t = 0; t < 5; t++) {
        f32x4 o = __builtin_bit_cast(f32x4, lds128(xb + (unsigned)(((w ^ 4) * 5 + t) * 1024 + lane * 16)));
        res[t] = (kh ? acc[1][t] : acc[0][t]) + o;
    }
}

DI void feat_map(u32x4 bm, u32x2 bl, unsigned wm, unsigned wl, unsigned (&ph)[9]) {
    float p[20];
#pragma unroll
    for (int u = 0; u < 5; u++) {
        f32x4 c = mfma32(lds128(wm + u * 1024), bm, f32x4{0, 0, 0, 0});
        c = mfma16(lds64(wl + u * 512), bl, c);
        p[4 * u] = c[0]; p[4 * u + 1] = c[1]; p[4 * u + 2] = c[2]; p[4 * u + 3] = c[3];
    }
    float m = p[0];
#pragma unroll
    for (int f = 1; f < 18; f++) m = fmaxf(m, p[f]);
    const float L2E = 1.44269504088896341f;
    float nm = -m * L2E, s = 0.f;
#pragma unroll
    for (int f = 0; f < 18; f++) { p[f] = __builtin_amdgcn_exp2f(fmaf(p[f], L2E, nm)); s += p[f]; }
    float inv = __builtin_amdgcn_rcpf(s);
#pragma unroll
    for (int f = 0; f < 9; f++) ph[f] = pk(p[2 * f] * inv, p[2 * f + 1] * inv);
}

__global__ __launch_bounds__(512, 2) void fused_kernel(Params P) {
    __shared__ __attribute__((aligned(1024))) char smem[163840];
    const unsigned sb = (unsigned)(size_t)(LAS char*)smem;
    STAMP(0);
    const int tid = threadIdx.x, lane = tid & 63, w = uni(tid >> 6);
    const int b = blockIdx.x, xcd = b & 7, r = b >> 3;
    const int r4 = r & 3, rq = (r >> 2) & 3;
    const int Tt = (xcd & 1) * 4 + rq;
    const int R = 2 * Tt + ((xcd >> 1) & 1), Cc = (xcd >> 2) * 4 + r4;
    const int pair = xcd * 16 + (r & 15);
    const rsrc_t ws = __builtin_amdgcn_make_buffer_rsrc(P.ws, 0, WS_END, 0x00020000);
    u64 ep = *(const __attribute__((address_space(4))) u64*)(size_t)(P.ws + WS_EPOCH);
    const int q4 = lane >> 4, l15 = lane & 15;
    const unsigned vrow = (unsigned)((lane >> 3) * 1152 + (((lane & 7) ^ ((lane >> 4) & 3)) << 4));
    const int mq = w & 3, kh = w >> 2;
    const int rt0 = 8 * R + 2 * mq;

    if (r < 16) {
        const int j = (xcd >> 1) * 4 + r4;
        constexpr unsigned STG = 49152, A_WF = 147456, A_WL = A_WF + 5120, A_RAW = A_WF + 10240, A_KVS = A_RAW + 2048;
        constexpr unsigned A_IMG = 82944;
        const int offs = (0x7420 >> (4 * r4)) & 15, offt = (0x7420 >> (4 * rq)) & 15;
        int kc = (offs + offt + (xcd & 1) + (xcd >> 1)) % 9;
        unsigned vw[2];
#pragma unroll
        for (int u = 0; u < 2; u++) {
            int e = 64 * (w + 8 * u) + lane; if (e > 959) e = 959;
            int pos = e / 15, c = e - pos * 15; int part = c / 5, cc = c - part * 5;
            int pp = pos & 15, m = pp >> 1;
            int k = (pos & ~15) + 8 * (m >> 2) + 4 * (pp & 1) + (m & 3);
            vw[u] = (unsigned)(k * 3456 + part * 1152 + ((72 * j) & ~15) + cc * 16);
        }
        const char* xw = P.x + (size_t)(256 * Tt + 32 * w) * 1152;
        auto issue = [&](int kk, unsigned slot) {
            const char* xs = xw + kk * 128;
            const char* wsrc = P.wqkv + (size_t)kk * (64 * 3456);
            dma16(wsrc, vw[0], sb + slot + 32768 + w * 1024);
            if (w < 7) dma16(wsrc, vw[1], sb + slot + 32768 + (w + 8) * 1024);
#pragma unroll
            for (int u = 0; u < 4; u++) dma16(xs + u * 8 * 1152, vrow ^ ((u & 1) << 6), sb + slot + (4 * w + u) * 1024);
        };
        if (w < 2) {
            int e = w * 64 + lane; if (e > 80) e = 80;
            dma16(P.wphi + j * 1296, e * 16, sb + A_RAW + w * 1024);
        }
        issue(kc, 0);
        { int k1 = kc + 1; if (k1 >= 9) k1 -= 9; issue(k1, STG); }
        asm volatile("" : "+s"(ep));
        const u32x2 tag = {(unsigned)(ep + 1), (unsigned)((ep + 1) >> 32)};
        if (tid == 0) st64(ws, WS_START + b * 8, 0, tag);
        unsigned vo[4];
#pragma unroll
        for (int u = 0; u < 4; u++) {
            int n = (u & 1) ? 8 : w;
            int e = 64 * n + lane; int pos = e / 9, c = e - pos * 9;
            int ks = pos >> 5, pp = pos & 15, m = pp >> 1;
            int kq = 2 * ((pos >> 4) & 1) + (m >> 2), je = 4 * (pp & 1) + (m & 3);
            int k = (u < 2) ? 36 * ks + 18 * (kq >> 1) + 8 * (kq & 1) + je
                            : 36 * (8 * ks + 2 * kq + (je >> 2)) + 18 * ((je >> 1) & 1) + 16 + (je & 1);
            vo[u] = (unsigned)(k * 1152 + Cc * 144 + c * 16);
        }
        f32x4 acc[7][2];
#pragma unroll
        for (int i = 0; i < 7; i++) { acc[i][0] = f32x4{0, 0, 0, 0}; acc[i][1] = f32x4{0, 0, 0, 0}; }
        const int tq = (lane >> 2) & 3, tp = lane & 3;
        const unsigned trow = (unsigned)(16 * (q4 >> 1) + 2 * (4 * (q4 & 1) + tq));
        const unsigned ax = sb + (unsigned)((32 * w + l15) * 128 + ((q4 ^ ((lane >> 1) & 7)) << 4));
        const unsigned aw = sb + 32768 + trow * 240 + (j & 1) * 8 + tp * 8;
        const unsigned aw6 = sb + 32768 + trow * 240 + (j & 1) * 8 + 64 + 80 * (tp > 2 ? 2 : tp);
        unsigned slot = 0, nslot = 2 * STG;
        STAMP(1);
#pragma unroll 1
        for (int it = 0; it < 9; it++) {
            wait_vm<5>();
            barrier();
            if (it == 0) { STAMP(2); CSTART; }
            if (it < 7) { int k2 = kc + 2; if (k2 >= 9) k2 -= 9; if (!NO_DMA) issue(k2, nslot); }
            else if (it == 7) {
#pragma unroll
                for (int u = 0; u < 5; u++) dma16(P.wo + u * 72 * 1152, vo[0], sb + (9 * u + w) * 1024);
                if (w < 5) dma16(P.wo + w * 72 * 1152, vo[1], sb + (9 * w + 8) * 1024);
            } else {
#pragma unroll
                for (int u = 5; u < 8; u++) dma16(P.wo + u * 72 * 1152, vo[0], sb + (9 * u + w) * 1024);
                if (w >= 5) dma16(P.wo + w * 72 * 1152, vo[1], sb + (9 * w + 8) * 1024);
                dma16(P.wo, vo[2], sb + (72 + w) * 1024);
                if (w == 0) dma16(P.wo, vo[3], sb + 80 * 1024);
            }
            if (it == 1 && w < 5) {
                int oq = l15 >> 2, hh = oq >> 1, f = 4 * w + (l15 & 3);
                unsigned sg = (oq & 1) ? 0x8000u : 0u;
                unsigned v[12];
#pragma unroll
                for (int e = 0; e < 12; e++) {
                    int col = e < 4 ? 4 * q4 + e : (e < 8 ? 12 + 4 * q4 + e : 24 + e);
                    int hc = col >= 18 ? 1 : 0, d = col - 18 * hc;
                    bool ok = hc == hh && f < 18;
                    unsigned t = *(LAS u16*)(size_t)(sb + A_RAW + (ok ? (hc * 324 + d * 18 + f) * 2 : 0));
                    v[e] = ok ? (t ^ sg) : 0u;
                }
                sts128(sb + A_WF + w * 1024 + lane * 16, u32x4{v[0] | v[1] << 16, v[2] | v[3] << 16, v[4] | v[5] << 16, v[6] | v[7] << 16});
                u32x2 lo = {v[8] | v[9] << 16, v[10] | v[11] << 16}, z = {0u, 0u};
                sts64(sb + A_WL + w * 512 + lane * 8, q4 == 0 ? lo : z);
                sts64(sb + A_WL + 2560 + w * 512 + lane * 8, q4 == 1 ? lo : z);
            }
            if (!NO_CMP)
#pragma unroll
            for (int s = 0; s < 2; s++) {
                u32x4 xf[2];
#pragma unroll
                for (int c = 0; c < 2; c++) xf[c] = lds128((ax ^ (s << 6)) + slot + c * 2048);
#pragma unroll
                for (int i = 0; i < 7; i++) {
                    u32x4 wf = i < 6 ? ldstr2(aw + slot + s * 7680 + (i >> 1) * 80 + (i & 1) * 32, 240)
                                     : ldstr2(aw6 + slot + s * 7680, 240);
#pragma unroll
                    for (int c = 0; c < 2; c++) acc[i][c] = mfma32(wf, xf[c], acc[i][c]);
                }
            }
            kc++; if (kc >= 9) kc -= 9;
            nslot = slot; slot += STG; if (slot == 3 * STG) slot = 0;
        }
        STAMP(3); CEND;
        u32x4 bq[2]; u32x2 bl[2];
        unsigned phk[2][9];
#pragma unroll
        for (int c = 0; c < 2; c++) {
            bq[c] = u32x4{pk(acc[0][c][0], acc[0][c][1]), pk(acc[0][c][2], acc[0][c][3]), pk(acc[1][c][0], acc[1][c][1]), pk(acc[1][c][2], acc[1][c][3])};
            bl[c] = u32x2{pk(acc[6][c][0], acc[6][c][1]), pk(acc[6][c][2], acc[6][c][3])};
            u32x4 bk = {pk(acc[2][c][0], acc[2][c][1]), pk(acc[2][c][2], acc[2][c][3]), pk(acc[3][c][0], acc[3][c][1]), pk(acc[3][c][2], acc[3][c][3])};
            feat_map(bk, bl[c], sb + A_WF + lane * 16, sb + A_WL + 2560 + lane * 8, phk[c]);
        }
        barrier();
#pragma unroll
        for (int c = 0; c < 2; c++) {
            unsigned row = sb + A_IMG + (unsigned)((32 * w + 16 * c + l15) * 240);
#pragma unroll
            for (int m = 0; m < 4; m++) sts64(row + q4 * 40 + m * 8, u32x2{phk[c][2 * m], phk[c][2 * m + 1]});
            sts64(row + q4 * 40 + 32, u32x2{phk[c][8], 0u});
            sts64(row + 160 + q4 * 8, u32x2{pk(acc[4][c][0], acc[4][c][1]), pk(acc[4][c][2], acc[4][c][3])});
            sts64(row + 192 + q4 * 8, u32x2{pk(acc[5][c][0], acc[5][c][1]), pk(acc[5][c][2], acc[5][c][3])});
            if (q4 >= 2) sts64(row + 208 + q4 * 8, q4 == 2 ? bl[c] : u32x2{0x3f80u, 0u});
        }
        if (tid < 32) sts64(sb + A_KVS + 3648 + tid * 8, u32x2{0u, 0u});
        barrier();
        STAMP(4);
        if (w < 6) {
            const int hh = w >= 3 ? 1 : 0, ft = w - 3 * hh;
            const unsigned base = sb + A_IMG + trow * 240;
            const unsigned aa = base + hh * 80 + ft * 32 + ((ft == 2) ? (tp & 1) : tp) * 8;
            const unsigned ab0 = base + 160 + hh * 40 + tp * 8;
            const unsigned ab1 = base + 160 + (tp == 0 ? 32 : 72);
            f32x4 k0 = {0, 0, 0, 0}, k1 = {0, 0, 0, 0};
#pragma unroll
            for (int ks = 0; ks < 8; ks++) {
                u32x4 a = ldstr2(aa + ks * 7680, 240);
                k0 = mfma32(a, ldstr2(ab0 + ks * 7680, 240), k0);
                k1 = mfma32(a, ldstr2(ab1 + ks * 7680, 240), k1);
            }
            const int grp = 4 * ft + q4;
            const unsigned so = WS_KV + (unsigned)(j * 8 + Tt) * 6144u;
            if (grp < 10) {
                st128(ws, (unsigned)(((hh * 19 + l15 + 2 * hh) * 10 + grp) * 16), so, u32x4{pk(k0[0], k0[1]), tag[0], pk(k0[2], k0[3]), tag[0]});
                int d1 = l15 == 4 ? 18 : (hh ? l15 - 2 : l15 + 16);
                bool ok = l15 == 4 || (hh ? (l15 == 2 || l15 == 3) : l15 < 2);
                if (ok) st128(ws, (unsigned)(((hh * 19 + d1) * 10 + grp) * 16), so, u32x4{pk(k1[0], k1[1]), tag[0], pk(k1[2], k1[3]), tag[0]});
            }
        }
        STAMP(5);
        unsigned phq[2][9];
#pragma unroll
        for (int c = 0; c < 2; c++) feat_map(bq[c], bl[c], sb + A_WF + lane * 16, sb + A_WL + lane * 8, phq[c]);
        barrier();
#pragma unroll
        for (int u = 0; u < 7; u++) {
            int m = w + 8 * u;
            if (m < 36) dma_c(P, m, 0, Cc, vrow, sb + A_IMG);
            else if (m < 54) dma_c(P, m - 36, 2, Cc, vrow, sb + A_IMG + 36864);
        }
        STAMP(6);
        if (w < 6) {
            const bool act = tid < 380;
            const unsigned vo_ = act ? tid * 16 : 0;
            u32x4 g[8];
#pragma unroll 1
            for (int spin = 0; spin < SPIN_CAP; spin++) {
                asm volatile("" ::: "memory");
                bool ok = true;
#pragma unroll
                for (int t = 0; t < 8; t++) g[t] = ld128(ws, vo_, WS_KV + (unsigned)(j * 8 + t) * 6144u);
#pragma unroll
                for (int t = 0; t < 8; t++) ok = ok && g[t][1] == tag[0] && g[t][3] == tag[0];
                if (__all(ok)) break;
                __builtin_amdgcn_s_sleep(1);
            }
            float s0 = 0, s1 = 0, s2 = 0, s3 = 0;
#pragma unroll
            for (int t = 0; t < 8; t++) { s0 += bflo(g[t][0]); s1 += bfhi(g[t][0]); s2 += bflo(g[t][2]); s3 += bfhi(g[t][2]); }
            int row = tid / 5, c5 = tid - row * 5;
            if (act) sts64(sb + A_KVS + row * 48 + c5 * 8, u32x2{pk(s0, s1), pk(s2, s3)});
        }
        barrier();
        STAMP(7);
        {
            const int oq = l15 >> 2, hh = oq >> 1;
            const bool on = (q4 >> 1) == hh;
            f32x4 nm[2][3];
#pragma unroll
            for (int u = 0; u < 3; u++) {
                int sl = l15 & 3;
                int d = u < 2 ? 8 * (oq & 1) + 4 * u + sl : (sl < 3 ? 16 + sl : 18);
                unsigned a = sb + A_KVS + (on ? (unsigned)(((hh * 19 + d) * 2 + (q4 & 1)) * 48) : 3648u);
                u32x4 a0 = lds128(a), a1 = lds128(a + 16); u32x2 a2 = lds64(a + 32);
#pragma unroll
                for (int c = 0; c < 2; c++) {
                    f32x4 t = mfma32(a0, u32x4{phq[c][0], phq[c][1], phq[c][2], phq[c][3]}, f32x4{0, 0, 0, 0});
                    t = mfma32(a1, u32x4{phq[c][4], phq[c][5], phq[c][6], phq[c][7]}, t);
                    nm[c][u] = mfma16(a2, u32x2{phq[c][8], 0u}, t);
                }
            }
#pragma unroll
            for (int c = 0; c < 2; c++) {
                float inv = __builtin_amdgcn_rcpf(nm[c][2][2] + 1e-6f);
                unsigned rt = (unsigned)(16 * Tt + 2 * w + c);
                st128(ws, lane * 16, WS_O + (rt * 16 + j) * 1024u,
                      u32x4{pk(nm[c][0][0] * inv, nm[c][0][1] * inv), pk(nm[c][0][2] * inv, nm[c][0][3] * inv),
                            pk(nm[c][1][0] * inv, nm[c][1][1] * inv), pk(nm[c][1][2] * inv, nm[c][1][3] * inv)});
                if (!(q4 & 1)) st32(ws, l15 * 8 + (q4 >> 1) * 4, WS_OL + (rt * 16 + j) * 128u, pk(nm[c][2][0] * inv, nm[c][2][1] * inv));
            }
        }
        wait_vm<0>();
        if (w == 1) wait_h(ws, R, 0, 3, lane, tag);
        barrier();
        if (tid == 0) st64(ws, WS_OFLAG + (unsigned)(Tt * 16 + j) * 128u, 0, tag);
        STAMP(8);
        f32x4 ya[2][5], yb[2][5];
#pragma unroll
        for (int t = 0; t < 5; t++) { ya[0][t] = ya[1][t] = yb[0][t] = yb[1][t] = f32x4{0, 0, 0, 0}; }
        p2_ssm<6>(ws, ya, rt0, 6 * kh, (r4 * 3) >> 1, 0, sb + A_IMG, lane);
        STAMP(9);
        if (w == 0) poll_flags(ws, WS_OFLAG + (unsigned)(Tt * 16 + l15) * 128u, true, tag);
        barrier();
        STAMP(10);
        {
            u32x4 of[9][2];
            const int rot = 2 * r4;
#pragma unroll
            for (int i = 0; i < 8; i++) {
                int ks = 8 * kh + ((i + rot) & 7);
#pragma unroll
                for (int m = 0; m < 2; m++) of[i][m] = ld128(ws, lane * 16, WS_O + (unsigned)((rt0 + m) * 16 + ks) * 1024u);
            }
#pragma unroll
            for (int m = 0; m < 2; m++) {
                unsigned so = WS_OL + (unsigned)((rt0 + m) * 16 + 8 * kh) * 128u;
                u32x2 lo = ld64(ws, q4 * 256 + l15 * 8, so), hi = ld64(ws, q4 * 256 + 128 + l15 * 8, so);
                of[8][m] = u32x4{lo[0], lo[1], hi[0], hi[1]};
            }
            const unsigned ab = sb + trow * 144 + tp * 8;
#pragma unroll
            for (int i = 0; i < 9; i++) {
                int ks = i < 8 ? 8 * kh + ((i + rot) & 7) : 16 + kh;
                unsigned a = ab + (unsigned)(ks * 4608);
#pragma unroll
                for (int t = 0; t < 5; t++) {
                    u32x4 wf = ldstr2(a + t * 32, 144);
#pragma unroll
                    for (int m = 0; m < 2; m++) yb[m][t] = mfma32(wf, of[i][m], yb[m][t]);
                }
            }
        }
        STAMP(11);
        float gt[2];
        u32x4 yv[5];
#pragma unroll 1
        for (int spin = 0; spin < SPIN_CAP; spin++) {
            asm volatile("" ::: "memory");
            u32x2 g0 = ld64(ws, l15 * 8, WS_G + (unsigned)rt0 * 128u), g1 = ld64(ws, l15 * 8 + 128, WS_G + (unsigned)rt0 * 128u);
#pragma unroll
            for (int t = 0; t < 5; t++) yv[t] = ld128(ws, lane * 16, WS_YA + (unsigned)((pair * 8 + w) * 5 + t) * 1024u);
            bool ok = g0[1] == tag[0] && g1[1] == tag[0];
#pragma unroll
            for (int t = 0; t < 5; t++) ok = ok && yv[t][1] == tag[0] && yv[t][3] == tag[0];
            gt[0] = __uint_as_float(g0[0]); gt[1] = __uint_as_float(g1[0]);
            if (__all(ok)) break;
            __builtin_amdgcn_s_sleep(1);
        }
        STAMP(12);
#pragma unroll
        for (int m = 0; m < 2; m++)
#pragma unroll
            for (int t = 0; t < 5; t++) yb[m][t] = gt[m] * ya[m][t] + (1.f - gt[m]) * yb[m][t];
        barrier();
        f32x4 res[5];
        khalf_reduce(yb, res, sb, w, lane);
        const float g = kh ? gt[1] : gt[0];
        float* orow = P.out + (size_t)(16 * (rt0 + kh) + l15) * 576 + 72 * Cc + 4 * q4;
#pragma unroll
        for (int t = 0; t < 5; t++) {
            f32x4 o = res[t] + g * f32x4{bflo(yv[t][0]), bfhi(yv[t][0]), bflo(yv[t][2]), bfhi(yv[t][2])};
            if (t < 4 || q4 < 2) *(GAS f32x4*)(orow + 16 * t) = o;
        }
        STAMP(13);
    } else {
        constexpr unsigned STG = 36864, S_TR = 110592, S_AGG = S_TR + 8 * 5376;
        const int half = xcd & 1, u = (xcd >> 1) * 16 + (r - 16);
        const int nt = half ? 6 : 7;
        const int g = half ? u / 6 : u / 7, tt = u - g * nt + (half ? 7 : 0);
        asm volatile("" : "+s"(ep));
        const u32x2 tag = {(unsigned)(ep + 1), (unsigned)((ep + 1) >> 32)};
        if (g < 9) {
            const int th = w >> 2, pg = w & 3;
            int kc = (tt * 4 + g * 2 + 1) % 9;
            unsigned vx[3];
#pragma unroll
            for (int e = 0; e < 3; e++) {
                int n = w + 8 * e; int tile = n >> 1; int h2 = tile >= 5 ? 1 : 0, i = tile - 5 * h2;
                int t = 160 * tt + 80 * h2 + 4 * i + 40 * (n & 1) + 20 * (lane >> 5) + ((lane >> 3) & 3);
                if (t > 2047) t = 2047;
                vx[e] = (unsigned)(t * 1152 + (((lane & 7) ^ ((lane >> 4) & 3) ^ ((n & 1) << 2)) << 4));
            }
            const char* bw = (((w >> 1) & 1) ? P.bim : P.bre) + (size_t)(64 * g + 16 * (w >> 2) + 8 * (w & 1)) * 1152;
            const unsigned vb = vrow ^ ((w & 1) << 6);
            auto issue = [&](int kk, unsigned slot) {
                const char* xs = P.x + kk * 128;
                dma16(bw + kk * 128, vb, sb + slot + 20480 + w * 1024);
                dma16(bw + kk * 128 + 32 * 1152, vb, sb + slot + 20480 + (w + 8) * 1024);
                dma16(xs, vx[0], sb + slot + w * 1024);
                dma16(xs, vx[1], sb + slot + (w + 8) * 1024);
                if (w < 4) dma16(xs, vx[2], sb + slot + (w + 16) * 1024);
            };
            const int p = 64 * g + 16 * pg + l15;
            unsigned r_a = *(const GAS u16*)(P.lre + p * 2), r_b = *(const GAS u16*)(P.lim + p * 2), r_d = *(const GAS u16*)(P.logdt + p * 2);
            issue(kc, 0);
            { int k1 = kc + 1; if (k1 >= 9) k1 -= 9; issue(k1, STG); }
            if (tid == 0) st64(ws, WS_START + b * 8, 0, tag);
            f32x4 acc[5][2];
#pragma unroll
            for (int i = 0; i < 5; i++) { acc[i][0] = f32x4{0, 0, 0, 0}; acc[i][1] = f32x4{0, 0, 0, 0}; }
            const unsigned sw = (unsigned)(l15 * 128 + ((q4 ^ ((lane >> 1) & 7)) << 4));
            const unsigned ax = sb + th * 10240 + sw, ab = sb + 20480 + pg * 4096 + sw;
            unsigned slot = 0, nslot = 2 * STG;
            STAMP(1);
#pragma unroll 1
            for (int it = 0; it < 9; it++) {
                wait_vm<4>();
                barrier();
                if (it == 0) { STAMP(2); CSTART; }
                if (it < 7) { int k2 = kc + 2; if (k2 >= 9) k2 -= 9; if (!NO_DMA) issue(k2, nslot); }
                else {
#pragma unroll
                    for (int e = 0; e < 5; e++) { int m = w + 8 * e; if (m < 36) dma_c(P, m, 3 + 2 * (it - 7), Cc, vrow, sb + nslot); }
                }
                if (!NO_CMP)
#pragma unroll
                for (int s = 0; s < 2; s++) {
                    u32x4 bf[2];
#pragma unroll
                    for (int c = 0; c < 2; c++) bf[c] = lds128((ab ^ (s << 6)) + slot + c * 2048);
#pragma unroll
                    for (int i = 0; i < 5; i++) {
                        u32x4 xf = lds128((ax ^ (s << 6)) + slot + i * 2048);
#pragma unroll
                        for (int c = 0; c < 2; c++) acc[i][c] = mfma32(xf, bf[c], acc[i][c]);
                    }
                }
                kc++; if (kc >= 9) kc -= 9;
                nslot = slot; slot += STG; if (slot == 3 * STG) slot = 0;
            }
            STAMP(3); CEND;
            f32x2 lam, coef;
            {
                float a = bf2f(r_a), bb = bf2f(r_b);
                float dt = bflo(pk(expf(bf2f(r_d)), 0.f));
                float e = expf(dt * a), sn, cs;
                sincosf(dt * bb, &sn, &cs);
                lam = f32x2{e * cs, e * sn};
                float nr = lam[0] - 1.f, ni = lam[1], inv = 2.f / (a * a + bb * bb);
                coef = f32x2{(nr * a + ni * bb) * inv, (ni * a - nr * bb) * inv};
            }
            f32x2 e = {0.f, 0.f};
#pragma unroll
            for (int i = 0; i < 5; i++)
#pragma unroll
                for (int k = 0; k < 4; k++) e = cfma(lam, e, f32x2{acc[i][0][k], acc[i][1][k]});
            const int seg = th * 4 + q4;
            sts64(sb + S_AGG + seg * 512 + (pg * 16 + l15) * 8, __builtin_bit_cast(u32x2, e));
            f32x2 l2 = cmul(lam, lam), l4 = cmul(l2, l2), l5 = cmul(l4, lam), l10 = cmul(l5, l5), l20 = cmul(l10, l10);
            barrier();
#pragma unroll
            for (int e2 = 0; e2 < 5; e2++) { int m = w + 8 * e2; if (m < 36) dma_c(P, m, 7, Cc, vrow, sb + 2 * STG); }
            f32x2 pre = {0.f, 0.f}, pw = {1.f, 0.f}, run = {0.f, 0.f}, rpw = {1.f, 0.f};
#pragma unroll
            for (int s = 0; s < 8; s++) {
                if (s == seg) { pre = run; pw = rpw; }
                run = cfma(l20, run, __builtin_bit_cast(f32x2, lds64(sb + S_AGG + s * 512 + (pg * 16 + l15) * 8)));
                rpw = cmul(rpw, l20);
            }
            if (th == 0 && q4 == 0)
                st128(ws, (pg * 16 + l15) * 16, WS_E + (unsigned)(tt * 9 + g) * 1024u,
                      u32x4{__float_as_uint(run[0]), tag[0], __float_as_uint(run[1]), tag[0]});
            STAMP(4);
            const f32x2 l160 = rpw, l320 = cmul(l160, l160), l640 = cmul(l320, l320);
            f32x2 wq = (q4 & 1) ? l160 : f32x2{1.f, 0.f};
            if (q4 & 2) wq = cmul(wq, l320);
#pragma unroll
            for (int i = 0; i < 5; i++)
#pragma unroll
                for (int k = 0; k < 4; k++) {
                    float ur = acc[i][0][k], ui = acc[i][1][k];
                    acc[i][0][k] = coef[0] * ur - coef[1] * ui;
                    acc[i][1][k] = -coef[0] * ui - coef[1] * ur;
                }
            f32x2 ev[3];
#pragma unroll 1
            for (int spin = 0; spin < SPIN_CAP; spin++) {
                asm volatile("" ::: "memory");
                bool ok = true;
#pragma unroll
                for (int m = 0; m < 3; m++) {
                    int t2 = tt - 1 - q4 - 4 * m;
                    bool need = t2 >= 0;
                    u32x4 v = ld128(ws, (unsigned)((need ? t2 : 0) * 9216 + (pg * 16 + l15) * 16), WS_E + (unsigned)g * 1024u);
                    ok = ok && (!need || (v[1] == tag[0] && v[3] == tag[0]));
                    ev[m] = need ? f32x2{__uint_as_float(v[0]), __uint_as_float(v[2])} : f32x2{0.f, 0.f};
                }
                if (__all(ok)) break;
                __builtin_amdgcn_s_sleep(1);
            }
            STAMP(5);
            f32x2 cr = cmul(wq, cfma(l640, cfma(l640, ev[2], ev[1]), ev[0]));
            cr[0] += __shfl_xor(cr[0], 16); cr[1] += __shfl_xor(cr[1], 16);
            cr[0] += __shfl_xor(cr[0], 32); cr[1] += __shfl_xor(cr[1], 32);
            f32x2 h = cmul(coef, cfma(pw, cr, pre));
            h[1] = -h[1];
            const f32x2 lc = {lam[0], -lam[1]};
            const unsigned tb = sb + S_TR + w * 5376;
#pragma unroll
            for (int i = 0; i < 5; i++) {
                float hr[4], hi[4];
#pragma unroll
                for (int k = 0; k < 4; k++) { h = cfma(lc, h, f32x2{acc[i][0][k], acc[i][1][k]}); hr[k] = h[0]; hi[k] = h[1]; }
                sts64(tb + l15 * 168 + q4 * 40 + i * 8, u32x2{pk(hr[0], hr[1]), pk(hr[2], hr[3])});
                sts64(tb + (16 + l15) * 168 + q4 * 40 + i * 8, u32x2{pk(hi[0], hi[1]), pk(hi[2], hi[3])});
            }
            wait_lgkm();
            const unsigned ta = tb + (8 * q4 + ((lane >> 2) & 3)) * 168 + (lane & 3) * 8;
#pragma unroll
            for (int i = 0; i < 5; i++) {
                u32x4 f = ldstr2(ta + i * 32, 4 * 168);
                int rt = 10 * tt + 5 * th + i;
                if (rt < 128) st128(ws, lane * 16, WS_H + (unsigned)(rt * 36 + 4 * g + pg) * 1024u, f);
            }
            wait_vm<0>();
            barrier();
            if (tid == 0) st64(ws, WS_HFLAG + (unsigned)(tt * 9 + g) * 128u, 0, tag);
            STAMP(6);
        } else {
            const int sp = half ? u - 53 : 0;
            if (tid == 0) st64(ws, WS_START + b * 8, 0, tag);
            u32x4 gw[9];
#pragma unroll
            for (int k = 0; k < 9; k++) gw[k] = *(const GAS u32x4*)(P.gw + k * 128 + (lane & 7) * 16);
            for (int rg = sp * 8 + w; rg < 256; rg += 88) {
                const char* xr = P.x + (size_t)(rg * 8 + (lane >> 3)) * 1152 + (lane & 7) * 16;
                u32x4 xv[9];
#pragma unroll
                for (int k = 0; k < 9; k++) xv[k] = *(const GAS u32x4*)(xr + k * 128);
                float s = 0.f;
#pragma unroll
                for (int k = 0; k < 9; k++)
#pragma unroll
                    for (int e = 0; e < 4; e++) s = fmaf(bfhi(xv[k][e]), bfhi(gw[k][e]), fmaf(bflo(xv[k][e]), bflo(gw[k][e]), s));
                s += __shfl_xor(s, 1); s += __shfl_xor(s, 2); s += __shfl_xor(s, 4);
                float gv = 1.f / (1.f + expf(-s));
                if ((lane & 7) == 0) st64(ws, (lane >> 3) * 8, WS_G + (unsigned)rg * 64u, u32x2{__float_as_uint(gv), tag[0]});
            }
#pragma unroll
            for (int e = 0; e < 14; e++) {
                int m = w + 8 * e;
                if (m < 108) { int pr = m / 36; dma_c(P, m - 36 * pr, 3 + 2 * pr, Cc, vrow, sb + pr * STG); }
            }
            if (sp == 0 && w == 0) {
#pragma unroll
                for (int e = 0; e < 4; e++) poll_flags(ws, WS_START + (unsigned)(e * 64 + lane) * 8u, true, tag);
                if (lane == 0) st64(ws, WS_EPOCH, 0, tag);
            }
            wait_vm<0>();
        }
        u32x2 xs[5], ds[5];
        {
            const char* xr = P.x + (size_t)(16 * (rt0 + kh) + l15) * 1152 + Cc * 144 + q4 * 8;
#pragma unroll
            for (int t = 0; t < 5; t++) {
                int o = (t < 4 || q4 < 2) ? t * 32 : 0;
                xs[t] = *(const GAS u32x2*)(xr + o);
                ds[t] = *(const GAS u32x2*)(P.dskip + Cc * 144 + q4 * 8 + o);
            }
        }
        if (w == 0) wait_h(ws, R, 3, 6, lane, tag);
        barrier();
        STAMP(7);
        f32x4 y[2][5];
#pragma unroll
        for (int t = 0; t < 5; t++) { y[0][t] = y[1][t] = f32x4{0, 0, 0, 0}; }
        p2_ssm<12>(ws, y, rt0, 12 * kh, 3 * r4, 3, sb, lane);
        STAMP(8);
        f32x4 res[5];
        khalf_reduce(y, res, sb + S_TR + 2048, w, lane);
#pragma unroll
        for (int t = 0; t < 5; t++) {
            float a0 = fmaf(bflo(xs[t][0]), bflo(ds[t][0]), res[t][0]), a1 = fmaf(bfhi(xs[t][0]), bfhi(ds[t][0]), res[t][1]);
            float a2 = fmaf(bflo(xs[t][1]), bflo(ds[t][1]), res[t][2]), a3 = fmaf(bfhi(xs[t][1]), bfhi(ds[t][1]), res[t][3]);
            st128(ws, lane * 16, WS_YA + (unsigned)((pair * 8 + w) * 5 + t) * 1024u, u32x4{pk(a0, a1), tag[0], pk(a2, a3), tag[0]});
        }
        STAMP(9);
    }
}

extern "C" void kernel_launch(void* const* d_in, const int* in_sizes, int n_in,
                              void* d_out, int out_size, void* d_ws, size_t ws_size,
                              hipStream_t stream) {
    Params P;
    P.x = (const char*)d_in[0];
    P.lre = (const char*)d_in[1];
    P.lim = (const char*)d_in[2];
    P.bre = (const char*)d_in[3];
    P.bim = (const char*)d_in[4];
    P.cre = (const char*)d_in[5];
    P.cim = (const char*)d_in[6];
    P.logdt = (const char*)d_in[7];
    P.dskip = (const char*)d_in[8];
    P.wqkv = (const char*)d_in[10];
    P.wphi = (const char*)d_in[11];
    P.wo = (const char*)d_in[12];
    P.gw = (const char*)d_in[13];
    P.out = (float*)d_out;
    P.ws = (char*)d_ws;
    fused_kernel<<<256, 512, 0, stream>>>(P);
}
```

```cpp
#define DBG 1
#define AMP_K 2
#define AMP_ROLE 0
#define AMP_NW 8
#include <hip/hip_runtime.h>
#include <stdint.h>

typedef unsigned short u16;
typedef unsigned long long u64;
typedef short s16x4 __attribute__((ext_vector_type(4)));
typedef __bf16 bf16x2 __attribute__((ext_vector_type(2)));
typedef __bf16 bf16x4 __attribute__((ext_vector_type(4)));
typedef __bf16 bf16x8 __attribute__((ext_vector_type(8)));
typedef float f32x2 __attribute__((ext_vector_type(2)));
typedef float f32x4 __attribute__((ext_vector_type(4)));
typedef unsigned u32x2 __attribute__((ext_vector_type(2)));
typedef unsigned u32x4 __attribute__((ext_vector_type(4)));
#define LAS __attribute__((address_space(3)))
#define GAS __attribute__((address_space(1)))
#define DI __device__ __forceinline__
typedef __amdgpu_buffer_rsrc_t rsrc_t;

constexpr unsigned WS_EPOCH = 0;
constexpr unsigned WS_START = 4096;
constexpr unsigned WS_HFLAG = 8192;
constexpr unsigned WS_OFLAG = 24576;
constexpr unsigned WS_G     = 40960;
constexpr unsigned WS_E     = 65536;
constexpr unsigned WS_KV    = 262144;
constexpr unsigned WS_OL    = 1114112;
constexpr unsigned WS_O     = 1376256;
constexpr unsigned WS_H     = WS_O + 128 * 16 * 1024;
constexpr unsigned WS_YA    = WS_H + 128 * 36 * 1024;
constexpr unsigned WS_END   = WS_YA + 128 * 40 * 1024;
constexpr unsigned WS_DBG   = 32u << 20;

constexpr int SPIN_CAP = 1 << 14;
constexpr int AUX_SC1 = 16;

struct Params {
    const char *x, *lre, *lim, *bre, *bim, *cre, *cim, *logdt, *dskip, *wqkv, *wphi, *wo, *gw;
    float* out;
    char* ws;
};

DI float bf2f(unsigned v) { return __uint_as_float(v << 16); }
DI float bflo(unsigned v) { return __uint_as_float(v << 16); }
DI float bfhi(unsigned v) { return __uint_as_float(v & 0xffff0000u); }
DI unsigned pk(float a, float b) { f32x2 t = {a, b}; return __builtin_bit_cast(unsigned, __builtin_convertvector(t, bf16x2)); }
DI f32x4 mfma32(u32x4 a, u32x4 b, f32x4 c) {
    return __builtin_amdgcn_mfma_f32_16x16x32_bf16(__builtin_bit_cast(bf16x8, a), __builtin_bit_cast(bf16x8, b), c, 0, 0, 0);
}
DI f32x4 mfma16(u32x2 a, u32x2 b, f32x4 c) {
    return mfma32(u32x4{a[0], a[1], 0u, 0u}, u32x4{b[0], b[1], 0u, 0u}, c);
}
DI u32x4 lds128(unsigned a) { return *(LAS u32x4*)(size_t)a; }
DI u32x2 lds64(unsigned a) { return *(LAS u32x2*)(size_t)a; }
DI void sts64(unsigned a, u32x2 v) { *(LAS u32x2*)(size_t)a = v; }
DI void sts128(unsigned a, u32x4 v) { *(LAS u32x4*)(size_t)a = v; }
DI u32x2 ldstr(unsigned a) {
    return __builtin_bit_cast(u32x2, __builtin_amdgcn_ds_read_tr16_b64_v4bf16((LAS bf16x4*)(size_t)a));
}
DI u32x4 ldstr2(unsigned a, unsigned d) { u32x2 lo = ldstr(a), hi = ldstr(a + d); return u32x4{lo[0], lo[1], hi[0], hi[1]}; }
DI void dma16(const char* sbase, unsigned voff, unsigned lds) {
    asm volatile("s_mov_b32 m0, %2\n\ts_nop 0\n\tglobal_load_lds_dwordx4 %0, %1" :: "v"(voff), "s"(sbase), "s"(lds) : "memory");
}
template <int N> DI void wait_vm() { asm volatile("s_waitcnt vmcnt(%0)" :: "i"(N) : "memory"); }
DI void wait_lgkm() { asm volatile("s_waitcnt lgkmcnt(0)" ::: "memory"); }
DI void barrier() { asm volatile("s_waitcnt lgkmcnt(0)\n\ts_barrier" ::: "memory"); }
DI int uni(int v) { return __builtin_amdgcn_readfirstlane(v); }
DI u32x4 ld128(rsrc_t r, unsigned v, unsigned s) { return __builtin_amdgcn_raw_buffer_load_b128(r, v, s, AUX_SC1); }
DI u32x2 ld64(rsrc_t r, unsigned v, unsigned s) { return __builtin_amdgcn_raw_buffer_load_b64(r, v, s, AUX_SC1); }
DI void st128(rsrc_t r, unsigned v, unsigned s, u32x4 d) { __builtin_amdgcn_raw_buffer_store_b128(d, r, v, s, AUX_SC1); }
DI void st64(rsrc_t r, unsigned v, unsigned s, u32x2 d) { __builtin_amdgcn_raw_buffer_store_b64(d, r, v, s, AUX_SC1); }
DI void st32(rsrc_t r, unsigned v, unsigned s, unsigned d) { __builtin_amdgcn_raw_buffer_store_b32(d, r, v, s, AUX_SC1); }
DI f32x2 cmul(f32x2 a, f32x2 b) { return f32x2{a[0] * b[0] - a[1] * b[1], a[0] * b[1] + a[1] * b[0]}; }
DI f32x2 cfma(f32x2 a, f32x2 b, f32x2 c) {
    return f32x2{fmaf(a[0], b[0], fmaf(-a[1], b[1], c[0])), fmaf(a[0], b[1], fmaf(a[1], b[0], c[1]))};
}

#define STAMP(i) do { stamp[i] = __builtin_amdgcn_s_memrealtime(); } while (0)
#define CSTART stamp[14] = __builtin_amdgcn_s_memtime()
#define CEND stamp[14] = __builtin_amdgcn_s_memtime() - stamp[14]
#define NO_DMA 0
#define NO_CMP 0

DI void poll_flags(rsrc_t ws, unsigned fo, bool need, u32x2 tag) {
#pragma unroll 1
    for (int spin = 0; spin < SPIN_CAP; spin++) {
        asm volatile("" ::: "memory");
        u32x2 f = ld64(ws, fo, 0);
        bool ok = !need || (f[0] == tag[0] && f[1] == tag[1]);
        if (__all(ok)) break;
        __builtin_amdgcn_s_sleep(1);
    }
    asm volatile("" ::: "memory");
}

DI void dma_c(const Params& P, int m, int g0, int Cc, unsigned vrow, unsigned lds) {
    int gi = m >= 18 ? 1 : 0; int mm = m - 18 * gi; int comp = mm >= 9 ? 1 : 0; int n = mm - 9 * comp;
    const char* base = (comp ? P.cim : P.cre) + (size_t)((72 * Cc + 8 * n) * 1152 + (g0 + gi) * 128);
    dma16(base, vrow ^ ((n & 1) << 6), lds + m * 1024);
}

DI void wait_h(rsrc_t ws, int R, int g0, int ng, int lane, u32x2 tag) {
    int t0 = (128 * R) / 160, t1 = (128 * R + 127) / 160;
    int tt = (lane >= 16) ? t1 : t0, g = lane & 15;
    bool need = lane < 32 && g < ng;
    unsigned fo = need ? WS_HFLAG + (unsigned)(tt * 9 + g0 + g) * 128u : WS_HFLAG;
    poll_flags(ws, fo, need, tag);
}

template <int NK>
DI void p2_ssm(rsrc_t ws, f32x4 (&acc)[2][5], int rt0, int k0, int rot, int gfirst, unsigned cbase, int lane) {
    u32x4 hf[NK][2];
#pragma unroll
    for (int i = 0; i < NK; i++) {
        int kk = i + rot; if (kk >= NK) kk -= NK;
        int ks = gfirst * 4 + k0 + kk;
#pragma unroll
        for (int m = 0; m < 2; m++) hf[i][m] = ld128(ws, lane * 16, WS_H + (unsigned)((rt0 + m) * 36 + ks) * 1024u);
    }
    int rho = lane & 15, kq = lane >> 4;
    unsigned lb = (unsigned)((kq >> 1) * 9216 + rho * 128 + (((kq & 1) ^ ((rho >> 1) & 7)) << 4));
#pragma unroll
    for (int i = 0; i < NK; i++) {
        int kk = i + rot; if (kk >= NK) kk -= NK;
        int kl = k0 + kk;
        unsigned a = (lb ^ (unsigned)((kl & 3) << 5)) + cbase + (unsigned)((kl >> 2) * 18432);
#pragma unroll
        for (int t = 0; t < 5; t++) {
            u32x4 w = lds128(a + t * 2048);
#pragma unroll
            for (int m = 0; m < 2; m++) acc[m][t] = mfma32(w, hf[i][m], acc[m][t]);
        }
    }
}

DI void khalf_reduce(f32x4 (&acc)[2][5], f32x4 (&res)[5], unsigned xb, int w, int lane) {
    int kh = w >> 2;
#pragma unroll
    for (int t = 0; t < 5; t++) {
        f32x4 s = kh ? acc[0][t] : acc[1][t];
        sts128(xb + (unsigned)((w * 5 + t) * 1024 + lane * 16), __builtin_bit_cast(u32x4, s));
    }
    barrier();
#pragma unroll
    for (int t = 0; t < 5; t++) {
        f32x4 o = __builtin_bit_cast(f32x4, lds128(xb + (unsigned)(((w ^ 4) * 5 + t) * 1024 + lane * 16)));
        res[t] = (kh ? acc[1][t] : acc[0][t]) + o;
    }
}

DI void feat_map(u32x4 bm, u32x2 bl, unsigned wm, unsigned wl, unsigned (&ph)[9]) {
    float p[20];
#pragma unroll
    for (int u = 0; u < 5; u++) {
        f32x4 c = mfma32(lds128(wm + u * 1024), bm, f32x4{0, 0, 0, 0});
        c = mfma16(lds64(wl + u * 512), bl, c);
        p[4 * u] = c[0]; p[4 * u + 1] = c[1]; p[4 * u + 2] = c[2]; p[4 * u + 3] = c[3];
    }
    float m = p[0];
#pragma unroll
    for (int f = 1; f < 18; f++) m = fmaxf(m, p[f]);
    const float L2E = 1.44269504088896341f;
    float nm = -m * L2E, s = 0.f;
#pragma unroll
    for (int f = 0; f < 18; f++) { p[f] = __builtin_amdgcn_exp2f(fmaf(p[f], L2E, nm)); s += p[f]; }
    float inv = __builtin_amdgcn_rcpf(s);
#pragma unroll
    for (int f = 0; f < 9; f++) ph[f] = pk(p[2 * f] * inv, p[2 * f + 1] * inv);
}

__global__ __launch_bounds__(512, 2) void fused_kernel(Params P) {
    __shared__ __attribute__((aligned(1024))) char smem[163840];
    const unsigned sb = (unsigned)(size_t)(LAS char*)smem;
    u64 stamp[16];
#pragma unroll
    for (int i = 0; i < 16; i++) stamp[i] = 0;
    STAMP(0);
    const int tid = threadIdx.x, lane = tid & 63, w = uni(tid >> 6);
    const int b = blockIdx.x, xcd = b & 7, r = b >> 3;
    const int r4 = r & 3, rq = (r >> 2) & 3;
    const int Tt = (xcd & 1) * 4 + rq;
    const int R = 2 * Tt + ((xcd >> 1) & 1), Cc = (xcd >> 2) * 4 + r4;
    const int pair = xcd * 16 + (r & 15);
    const rsrc_t ws = __builtin_amdgcn_make_buffer_rsrc(P.ws, 0, WS_END, 0x00020000);
    u64 ep = *(const __attribute__((address_space(4))) u64*)(size_t)(P.ws + WS_EPOCH);
    const int q4 = lane >> 4, l15 = lane & 15;
    const unsigned vrow = (unsigned)((lane >> 3) * 1152 + (((lane & 7) ^ ((lane >> 4) & 3)) << 4));
    const int mq = w & 3, kh = w >> 2;
    const int rt0 = 8 * R + 2 * mq;

    if (r < 16) {
        const int j = (xcd >> 1) * 4 + r4;
        constexpr unsigned STG = 49152, A_WF = 147456, A_WL = A_WF + 5120, A_RAW = A_WF + 10240, A_KVS = A_RAW + 2048;
        constexpr unsigned A_IMG = 82944;
        const int offs = (0x7420 >> (4 * r4)) & 15, offt = (0x7420 >> (4 * rq)) & 15;
        int kc = (offs + offt + (xcd & 1) + (xcd >> 1)) % 9;
        unsigned vw[2];
#pragma unroll
        for (int u = 0; u < 2; u++) {
            int e = 64 * (w + 8 * u) + lane; if (e > 959) e = 959;
            int pos = e / 15, c = e - pos * 15; int part = c / 5, cc = c - part * 5;
            int pp = pos & 15, m = pp >> 1;
            int k = (pos & ~15) + 8 * (m >> 2) + 4 * (pp & 1) + (m & 3);
            vw[u] = (unsigned)(k * 3456 + part * 1152 + ((72 * j) & ~15) + cc * 16);
        }
        const char* xw = P.x + (size_t)(256 * Tt + 32 * w) * 1152;
        auto issue = [&](int kk, unsigned slot) {
            const char* xs = xw + kk * 128;
            const char* wsrc = P.wqkv + (size_t)kk * (64 * 3456);
            dma16(wsrc, vw[0], sb + slot + 32768 + w * 1024);
            if (w < 7) dma16(wsrc, vw[1], sb + slot + 32768 + (w + 8) * 1024);
#pragma unroll
            for (int u = 0; u < 4; u++) dma16(xs + u * 8 * 1152, vrow ^ ((u & 1) << 6), sb + slot + (4 * w + u) * 1024);
        };
        if (w < 2) {
            int e = w * 64 + lane; if (e > 80) e = 80;
            dma16(P.wphi + j * 1296, e * 16, sb + A_RAW + w * 1024);
        }
        issue(kc, 0);
        { int k1 = kc + 1; if (k1 >= 9) k1 -= 9; issue(k1, STG); }
        asm volatile("" : "+s"(ep));
        const u32x2 tag = {(unsigned)(ep + 1), (unsigned)((ep + 1) >> 32)};
        if (tid == 0) st64(ws, WS_START + b * 8, 0, tag);
        unsigned vo[4];
#pragma unroll
        for (int u = 0; u < 4; u++) {
            int n = (u & 1) ? 8 : w;
            int e = 64 * n + lane; int pos = e / 9, c = e - pos * 9;
            int ks = pos >> 5, pp = pos & 15, m = pp >> 1;
            int kq = 2 * ((pos >> 4) & 1) + (m >> 2), je = 4 * (pp & 1) + (m & 3);
            int k = (u < 2) ? 36 * ks + 18 * (kq >> 1) + 8 * (kq & 1) + je
                            : 36 * (8 * ks + 2 * kq + (je >> 2)) + 18 * ((je >> 1) & 1) + 16 + (je & 1);
            vo[u] = (unsigned)(k * 1152 + Cc * 144 + c * 16);
        }
        f32x4 acc[7][2];
#pragma unroll
        for (int i = 0; i < 7; i++) { acc[i][0] = f32x4{0, 0, 0, 0}; acc[i][1] = f32x4{0, 0, 0, 0}; }
        const int tq = (lane >> 2) & 3, tp = lane & 3;
        const unsigned trow = (unsigned)(16 * (q4 >> 1) + 2 * (4 * (q4 & 1) + tq));
        const unsigned ax = sb + (unsigned)((32 * w + l15) * 128 + ((q4 ^ ((lane >> 1) & 7)) << 4));
        const unsigned aw = sb + 32768 + trow * 240 + (j & 1) * 8 + tp * 8;
        const unsigned aw6 = sb + 32768 + trow * 240 + (j & 1) * 8 + 64 + 80 * (tp > 2 ? 2 : tp);
        unsigned slot = 0, nslot = 2 * STG;
        STAMP(1);
#pragma unroll 1
        for (int it = 0; it < 9; it++) {
            wait_vm<5>();
            barrier();
            if (it == 0) { STAMP(2); CSTART; }
            if (it < 7) { int k2 = kc + 2; if (k2 >= 9) k2 -= 9; if (!NO_DMA) issue(k2, nslot); }
            else if (it == 7) {
#pragma unroll
                for (int u = 0; u < 5; u++) dma16(P.wo + u * 72 * 1152, vo[0], sb + (9 * u + w) * 1024);
                if (w < 5) dma16(P.wo + w * 72 * 1152, vo[1], sb + (9 * w + 8) * 1024);
            } else {
#pragma unroll
                for (int u = 5; u < 8; u++) dma16(P.wo + u * 72 * 1152, vo[0], sb + (9 * u + w) * 1024);
                if (w >= 5) dma16(P.wo + w * 72 * 1152, vo[1], sb + (9 * w + 8) * 1024);
                dma16(P.wo, vo[2], sb + (72 + w) * 1024);
                if (w == 0) dma16(P.wo, vo[3], sb + 80 * 1024);
            }
            if (it == 1 && w < 5) {
                int oq = l15 >> 2, hh = oq >> 1, f = 4 * w + (l15 & 3);
                unsigned sg = (oq & 1) ? 0x8000u : 0u;
                unsigned v[12];
#pragma unroll
                for (int e = 0; e < 12; e++) {
                    int col = e < 4 ? 4 * q4 + e : (e < 8 ? 12 + 4 * q4 + e : 24 + e);
                    int hc = col >= 18 ? 1 : 0, d = col - 18 * hc;
                    bool ok = hc == hh && f < 18;
                    unsigned t = *(LAS u16*)(size_t)(sb + A_RAW + (ok ? (hc * 324 + d * 18 + f) * 2 : 0));
                    v[e] = ok ? (t ^ sg) : 0u;
                }
                sts128(sb + A_WF + w * 1024 + lane * 16, u32x4{v[0] | v[1] << 16, v[2] | v[3] << 16, v[4] | v[5] << 16, v[6] | v[7] << 16});
                u32x2 lo = {v[8] | v[9] << 16, v[10] | v[11] << 16}, z = {0u, 0u};
                sts64(sb + A_WL + w * 512 + lane * 8, q4 == 0 ? lo : z);
                sts64(sb + A_WL + 2560 + w * 512 + lane * 8, q4 == 1 ? lo : z);
            }
            if (!NO_CMP)
#pragma unroll
            for (int s = 0; s < 2; s++) {
                u32x4 xf[2];
#pragma unroll
                for (int c = 0; c < 2; c++) xf[c] = lds128((ax ^ (s << 6)) + slot + c * 2048);
#pragma unroll
                for (int i = 0; i < 7; i++) {
                    u32x4 wf = i < 6 ? ldstr2(aw + slot + s * 7680 + (i >> 1) * 80 + (i & 1) * 32, 240)
                                     : ldstr2(aw6 + slot + s * 7680, 240);
#pragma unroll
                    for (int c = 0; c < 2; c++) acc[i][c] = mfma32(wf, xf[c], acc[i][c]);
                }
            }
            kc++; if (kc >= 9) kc -= 9;
            nslot = slot; slot += STG; if (slot == 3 * STG) slot = 0;
        }
        STAMP(3); CEND;
        u32x4 bq[2]; u32x2 bl[2];
        unsigned phk[2][9];
#pragma unroll
        for (int c = 0; c < 2; c++) {
            bq[c] = u32x4{pk(acc[0][c][0], acc[0][c][1]), pk(acc[0][c][2], acc[0][c][3]), pk(acc[1][c][0], acc[1][c][1]), pk(acc[1][c][2], acc[1][c][3])};
            bl[c] = u32x2{pk(acc[6][c][0], acc[6][c][1]), pk(acc[6][c][2], acc[6][c][3])};
            u32x4 bk = {pk(acc[2][c][0], acc[2][c][1]), pk(acc[2][c][2], acc[2][c][3]), pk(acc[3][c][0], acc[3][c][1]), pk(acc[3][c][2], acc[3][c][3])};
            feat_map(bk, bl[c], sb + A_WF + lane * 16, sb + A_WL + 2560 + lane * 8, phk[c]);
        }
        barrier();
#pragma unroll
        for (int c = 0; c < 2; c++) {
            unsigned row = sb + A_IMG + (unsigned)((32 * w + 16 * c + l15) * 240);
#pragma unroll
            for (int m = 0; m < 4; m++) sts64(row + q4 * 40 + m * 8, u32x2{phk[c][2 * m], phk[c][2 * m + 1]});
            sts64(row + q4 * 40 + 32, u32x2{phk[c][8], 0u});
            sts64(row + 160 + q4 * 8, u32x2{pk(acc[4][c][0], acc[4][c][1]), pk(acc[4][c][2], acc[4][c][3])});
            sts64(row + 192 + q4 * 8, u32x2{pk(acc[5][c][0], acc[5][c][1]), pk(acc[5][c][2], acc[5][c][3])});
            if (q4 >= 2) sts64(row + 208 + q4 * 8, q4 == 2 ? bl[c] : u32x2{0x3f80u, 0u});
        }
        if (tid < 32) sts64(sb + A_KVS + 3648 + tid * 8, u32x2{0u, 0u});
        barrier();
        STAMP(4);
        if (w < 6) {
            const int hh = w >= 3 ? 1 : 0, ft = w - 3 * hh;
            const unsigned base = sb + A_IMG + trow * 240;
            const unsigned aa = base + hh * 80 + ft * 32 + ((ft == 2) ? (tp & 1) : tp) * 8;
            const unsigned ab0 = base + 160 + hh * 40 + tp * 8;
            const unsigned ab1 = base + 160 + (tp == 0 ? 32 : 72);
            f32x4 k0 = {0, 0, 0, 0}, k1 = {0, 0, 0, 0};
#pragma unroll
            for (int ks = 0; ks < 8; ks++) {
                u32x4 a = ldstr2(aa + ks * 7680, 240);
                k0 = mfma32(a, ldstr2(ab0 + ks * 7680, 240), k0);
                k1 = mfma32(a, ldstr2(ab1 + ks * 7680, 240), k1);
            }
            const int grp = 4 * ft + q4;
            const unsigned so = WS_KV + (unsigned)(j * 8 + Tt) * 6144u;
            if (grp < 10) {
                st128(ws, (unsigned)(((hh * 19 + l15 + 2 * hh) * 10 + grp) * 16), so, u32x4{pk(k0[0], k0[1]), tag[0], pk(k0[2], k0[3]), tag[0]});
                int d1 = l15 == 4 ? 18 : (hh ? l15 - 2 : l15 + 16);
                bool ok = l15 == 4 || (hh ? (l15 == 2 || l15 == 3) : l15 < 2);
                if (ok) st128(ws, (unsigned)(((hh * 19 + d1) * 10 + grp) * 16), so, u32x4{pk(k1[0], k1[1]), tag[0], pk(k1[2], k1[3]), tag[0]});
            }
        }
        STAMP(5);
        unsigned phq[2][9];
#pragma unroll
        for (int c = 0; c < 2; c++) feat_map(bq[c], bl[c], sb + A_WF + lane * 16, sb + A_WL + lane * 8, phq[c]);
        barrier();
#pragma unroll
        for (int u = 0; u < 7; u++) {
            int m = w + 8 * u;
            if (m < 36) dma_c(P, m, 0, Cc, vrow, sb + A_IMG);
            else if (m < 54) dma_c(P, m - 36, 2, Cc, vrow, sb + A_IMG + 36864);
        }
        STAMP(6);
        if (w < 6) {
            const bool act = tid < 380;
            const unsigned vo_ = act ? tid * 16 : 0;
            u32x4 g[8];
#pragma unroll 1
            for (int spin = 0; spin < SPIN_CAP; spin++) {
                asm volatile("" ::: "memory");
                bool ok = true;
#pragma unroll
                for (int t = 0; t < 8; t++) g[t] = ld128(ws, vo_, WS_KV + (unsigned)(j * 8 + t) * 6144u);
#pragma unroll
                for (int t = 0; t < 8; t++) ok = ok && g[t][1] == tag[0] && g[t][3] == tag[0];
                if (__all(ok)) break;
                __builtin_amdgcn_s_sleep(1);
            }
            float s0 = 0, s1 = 0, s2 = 0, s3 = 0;
#pragma unroll
            for (int t = 0; t < 8; t++) { s0 += bflo(g[t][0]); s1 += bfhi(g[t][0]); s2 += bflo(g[t][2]); s3 += bfhi(g[t][2]); }
            int row = tid / 5, c5 = tid - row * 5;
            if (act) sts64(sb + A_KVS + row * 48 + c5 * 8, u32x2{pk(s0, s1), pk(s2, s3)});
        }
        barrier();
        STAMP(7);
        {
            const int oq = l15 >> 2, hh = oq >> 1;
            const bool on = (q4 >> 1) == hh;
            f32x4 nm[2][3];
#pragma unroll
            for (int u = 0; u < 3; u++) {
                int sl = l15 & 3;
                int d = u < 2 ? 8 * (oq & 1) + 4 * u + sl : (sl < 3 ? 16 + sl : 18);
                unsigned a = sb + A_KVS + (on ? (unsigned)(((hh * 19 + d) * 2 + (q4 & 1)) * 48) : 3648u);
                u32x4 a0 = lds128(a), a1 = lds128(a + 16); u32x2 a2 = lds64(a + 32);
#pragma unroll
                for (int c = 0; c < 2; c++) {
                    f32x4 t = mfma32(a0, u32x4{phq[c][0], phq[c][1], phq[c][2], phq[c][3]}, f32x4{0, 0, 0, 0});
                    t = mfma32(a1, u32x4{phq[c][4], phq[c][5], phq[c][6], phq[c][7]}, t);
                    nm[c][u] = mfma16(a2, u32x2{phq[c][8], 0u}, t);
                }
            }
#pragma unroll
            for (int c = 0; c < 2; c++) {
                float inv = __builtin_amdgcn_rcpf(nm[c][2][2] + 1e-6f);
                unsigned rt = (unsigned)(16 * Tt + 2 * w + c);
                st128(ws, lane * 16, WS_O + (rt * 16 + j) * 1024u,
                      u32x4{pk(nm[c][0][0] * inv, nm[c][0][1] * inv), pk(nm[c][0][2] * inv, nm[c][0][3] * inv),
                            pk(nm[c][1][0] * inv, nm[c][1][1] * inv), pk(nm[c][1][2] * inv, nm[c][1][3] * inv)});
                if (!(q4 & 1)) st32(ws, l15 * 8 + (q4 >> 1) * 4, WS_OL + (rt * 16 + j) * 128u, pk(nm[c][2][0] * inv, nm[c][2][1] * inv));
            }
        }
        wait_vm<0>();
        if (w == 1) wait_h(ws, R, 0, 3, lane, tag);
        barrier();
        if (tid == 0) st64(ws, WS_OFLAG + (unsigned)(Tt * 16 + j) * 128u, 0, tag);
        STAMP(8);
        f32x4 ya[2][5], yb[2][5];
#pragma unroll
        for (int t = 0; t < 5; t++) { ya[0][t] = ya[1][t] = yb[0][t] = yb[1][t] = f32x4{0, 0, 0, 0}; }
        p2_ssm<6>(ws, ya, rt0, 6 * kh, (r4 * 3) >> 1, 0, sb + A_IMG, lane);
        STAMP(9);
        if (w == 0) poll_flags(ws, WS_OFLAG + (unsigned)(Tt * 16 + l15) * 128u, true, tag);
        barrier();
        STAMP(10);
        {
            u32x4 of[9][2];
            const int rot = 2 * r4;
#pragma unroll
            for (int i = 0; i < 8; i++) {
                int ks = 8 * kh + ((i + rot) & 7);
#pragma unroll
                for (int m = 0; m < 2; m++) of[i][m] = ld128(ws, lane * 16, WS_O + (unsigned)((rt0 + m) * 16 + ks) * 1024u);
            }
#pragma unroll
            for (int m = 0; m < 2; m++) {
                unsigned so = WS_OL + (unsigned)((rt0 + m) * 16 + 8 * kh) * 128u;
                u32x2 lo = ld64(ws, q4 * 256 + l15 * 8, so), hi = ld64(ws, q4 * 256 + 128 + l15 * 8, so);
                of[8][m] = u32x4{lo[0], lo[1], hi[0], hi[1]};
            }
            const unsigned ab = sb + trow * 144 + tp * 8;
#pragma unroll
            for (int i = 0; i < 9; i++) {
                int ks = i < 8 ? 8 * kh + ((i + rot) & 7) : 16 + kh;
                unsigned a = ab + (unsigned)(ks * 4608);
#pragma unroll
                for (int t = 0; t < 5; t++) {
                    u32x4 wf = ldstr2(a + t * 32, 144);
#pragma unroll
                    for (int m = 0; m < 2; m++) yb[m][t] = mfma32(wf, of[i][m], yb[m][t]);
                }
            }
        }
        STAMP(11);
        float gt[2];
        u32x4 yv[5];
#pragma unroll 1
        for (int spin = 0; spin < SPIN_CAP; spin++) {
            asm volatile("" ::: "memory");
            u32x2 g0 = ld64(ws, l15 * 8, WS_G + (unsigned)rt0 * 128u), g1 = ld64(ws, l15 * 8 + 128, WS_G + (unsigned)rt0 * 128u);
#pragma unroll
            for (int t = 0; t < 5; t++) yv[t] = ld128(ws, lane * 16, WS_YA + (unsigned)((pair * 8 + w) * 5 + t) * 1024u);
            bool ok = g0[1] == tag[0] && g1[1] == tag[0];
#pragma unroll
            for (int t = 0; t < 5; t++) ok = ok && yv[t][1] == tag[0] && yv[t][3] == tag[0];
            gt[0] = __uint_as_float(g0[0]); gt[1] = __uint_as_float(g1[0]);
            if (__all(ok)) break;
            __builtin_amdgcn_s_sleep(1);
        }
        STAMP(12);
#pragma unroll
        for (int m = 0; m < 2; m++)
#pragma unroll
            for (int t = 0; t < 5; t++) yb[m][t] = gt[m] * ya[m][t] + (1.f - gt[m]) * yb[m][t];
        barrier();
        f32x4 res[5];
        khalf_reduce(yb, res, sb, w, lane);
        const float g = kh ? gt[1] : gt[0];
        float* orow = P.out + (size_t)(16 * (rt0 + kh) + l15) * 576 + 72 * Cc + 4 * q4;
#pragma unroll
        for (int t = 0; t < 5; t++) {
            f32x4 o = res[t] + g * f32x4{bflo(yv[t][0]), bfhi(yv[t][0]), bflo(yv[t][2]), bfhi(yv[t][2])};
            if (t < 4 || q4 < 2) *(GAS f32x4*)(orow + 16 * t) = o;
        }
        STAMP(13);
    } else {
        constexpr unsigned STG = 36864, S_TR = 110592, S_AGG = S_TR + 8 * 5376;
        const int half = xcd & 1, u = (xcd >> 1) * 16 + (r - 16);
        const int nt = half ? 6 : 7;
        const int g = half ? u / 6 : u / 7, tt = u - g * nt + (half ? 7 : 0);
        asm volatile("" : "+s"(ep));
        const u32x2 tag = {(unsigned)(ep + 1), (unsigned)((ep + 1) >> 32)};
        if (g < 9) {
            const int th = w >> 2, pg = w & 3;
            int kc = (tt * 4 + g * 2 + 1) % 9;
            unsigned vx[3];
#pragma unroll
            for (int e = 0; e < 3; e++) {
                int n = w + 8 * e; int tile = n >> 1; int h2 = tile >= 5 ? 1 : 0, i = tile - 5 * h2;
                int t = 160 * tt + 80 * h2 + 4 * i + 40 * (n & 1) + 20 * (lane >> 5) + ((lane >> 3) & 3);
                if (t > 2047) t = 2047;
                vx[e] = (unsigned)(t * 1152 + (((lane & 7) ^ ((lane >> 4) & 3) ^ ((n & 1) << 2)) << 4));
            }
            const char* bw = (((w >> 1) & 1) ? P.bim : P.bre) + (size_t)(64 * g + 16 * (w >> 2) + 8 * (w & 1)) * 1152;
            const unsigned vb = vrow ^ ((w & 1) << 6);
            auto issue = [&](int kk, unsigned slot) {
                const char* xs = P.x + kk * 128;
                dma16(bw + kk * 128, vb, sb + slot + 20480 + w * 1024);
                dma16(bw + kk * 128 + 32 * 1152, vb, sb + slot + 20480 + (w + 8) * 1024);
                dma16(xs, vx[0], sb + slot + w * 1024);
                dma16(xs, vx[1], sb + slot + (w + 8) * 1024);
                if (w < 4) dma16(xs, vx[2], sb + slot + (w + 16) * 1024);
            };
            const int p = 64 * g + 16 * pg + l15;
            unsigned r_a = *(const GAS u16*)(P.lre + p * 2), r_b = *(const GAS u16*)(P.lim + p * 2), r_d = *(const GAS u16*)(P.logdt + p * 2);
            issue(kc, 0);
            { int k1 = kc + 1; if (k1 >= 9) k1 -= 9; issue(k1, STG); }
            if (tid == 0) st64(ws, WS_START + b * 8, 0, tag);
            f32x4 acc[5][2];
#pragma unroll
            for (int i = 0; i < 5; i++) { acc[i][0] = f32x4{0, 0, 0, 0}; acc[i][1] = f32x4{0, 0, 0, 0}; }
            const unsigned sw = (unsigned)(l15 * 128 + ((q4 ^ ((lane >> 1) & 7)) << 4));
            const unsigned ax = sb + th * 10240 + sw, ab = sb + 20480 + pg * 4096 + sw;
            unsigned slot = 0, nslot = 2 * STG;
            STAMP(1);
#pragma unroll 1
            for (int it = 0; it < 9; it++) {
                wait_vm<4>();
                barrier();
                if (it == 0) { STAMP(2); CSTART; }
                if (it < 7) { int k2 = kc + 2; if (k2 >= 9) k2 -= 9; if (!NO_DMA) issue(k2, nslot); }
                else {
#pragma unroll
                    for (int e = 0; e < 5; e++) { int m = w + 8 * e; if (m < 36) dma_c(P, m, 3 + 2 * (it - 7), Cc, vrow, sb + nslot); }
                }
                if (!NO_CMP)
#pragma unroll
                for (int s = 0; s < 2; s++) {
                    u32x4 bf[2];
#pragma unroll
                    for (int c = 0; c < 2; c++) bf[c] = lds128((ab ^ (s << 6)) + slot + c * 2048);
#pragma unroll
                    for (int i = 0; i < 5; i++) {
                        u32x4 xf = lds128((ax ^ (s << 6)) + slot + i * 2048);
#pragma unroll
                        for (int c = 0; c < 2; c++) acc[i][c] = mfma32(xf, bf[c], acc[i][c]);
                    }
                }
                kc++; if (kc >= 9) kc -= 9;
                nslot = slot; slot += STG; if (slot == 3 * STG) slot = 0;
            }
            STAMP(3); CEND;
            f32x2 lam, coef;
            {
                float a = bf2f(r_a), bb = bf2f(r_b);
                float dt = bflo(pk(expf(bf2f(r_d)), 0.f));
                float e = expf(dt * a), sn, cs;
                sincosf(dt * bb, &sn, &cs);
                lam = f32x2{e * cs, e * sn};
                float nr = lam[0] - 1.f, ni = lam[1], inv = 2.f / (a * a + bb * bb);
                coef = f32x2{(nr * a + ni * bb) * inv, (ni * a - nr * bb) * inv};
            }
            f32x2 e = {0.f, 0.f};
#pragma unroll
            for (int i = 0; i < 5; i++)
#pragma unroll
                for (int k = 0; k < 4; k++) e = cfma(lam, e, f32x2{acc[i][0][k], acc[i][1][k]});
            const int seg = th * 4 + q4;
            sts64(sb + S_AGG + seg * 512 + (pg * 16 + l15) * 8, __builtin_bit_cast(u32x2, e));
            f32x2 l2 = cmul(lam, lam), l4 = cmul(l2, l2), l5 = cmul(l4, lam), l10 = cmul(l5, l5), l20 = cmul(l10, l10);
            barrier();
#pragma unroll
            for (int e2 = 0; e2 < 5; e2++) { int m = w + 8 * e2; if (m < 36) dma_c(P, m, 7, Cc, vrow, sb + 2 * STG); }
            f32x2 pre = {0.f, 0.f}, pw = {1.f, 0.f}, run = {0.f, 0.f}, rpw = {1.f, 0.f};
#pragma unroll
            for (int s = 0; s < 8; s++) {
                if (s == seg) { pre = run; pw = rpw; }
                run = cfma(l20, run, __builtin_bit_cast(f32x2, lds64(sb + S_AGG + s * 512 + (pg * 16 + l15) * 8)));
                rpw = cmul(rpw, l20);
            }
            if (th == 0 && q4 == 0)
                st128(ws, (pg * 16 + l15) * 16, WS_E + (unsigned)(tt * 9 + g) * 1024u,
                      u32x4{__float_as_uint(run[0]), tag[0], __float_as_uint(run[1]), tag[0]});
            STAMP(4);
            const f32x2 l160 = rpw, l320 = cmul(l160, l160), l640 = cmul(l320, l320);
            f32x2 wq = (q4 & 1) ? l160 : f32x2{1.f, 0.f};
            if (q4 & 2) wq = cmul(wq, l320);
#pragma unroll
            for (int i = 0; i < 5; i++)
#pragma unroll
                for (int k = 0; k < 4; k++) {
                    float ur = acc[i][0][k], ui = acc[i][1][k];
                    acc[i][0][k] = coef[0] * ur - coef[1] * ui;
                    acc[i][1][k] = -coef[0] * ui - coef[1] * ur;
                }
            f32x2 ev[3];
#pragma unroll 1
            for (int spin = 0; spin < SPIN_CAP; spin++) {
                asm volatile("" ::: "memory");
                bool ok = true;
#pragma unroll
                for (int m = 0; m < 3; m++) {
                    int t2 = tt - 1 - q4 - 4 * m;
                    bool need = t2 >= 0;
                    u32x4 v = ld128(ws, (unsigned)((need ? t2 : 0) * 9216 + (pg * 16 + l15) * 16), WS_E + (unsigned)g * 1024u);
                    ok = ok && (!need || (v[1] == tag[0] && v[3] == tag[0]));
                    ev[m] = need ? f32x2{__uint_as_float(v[0]), __uint_as_float(v[2])} : f32x2{0.f, 0.f};
                }
                if (__all(ok)) break;
                __builtin_amdgcn_s_sleep(1);
            }
            STAMP(5);
            f32x2 cr = cmul(wq, cfma(l640, cfma(l640, ev[2], ev[1]), ev[0]));
            cr[0] += __shfl_xor(cr[0], 16); cr[1] += __shfl_xor(cr[1], 16);
            cr[0] += __shfl_xor(cr[0], 32); cr[1] += __shfl_xor(cr[1], 32);
            f32x2 h = cmul(coef, cfma(pw, cr, pre));
            h[1] = -h[1];
            const f32x2 lc = {lam[0], -lam[1]};
            const unsigned tb = sb + S_TR + w * 5376;
#pragma unroll
            for (int i = 0; i < 5; i++) {
                float hr[4], hi[4];
#pragma unroll
                for (int k = 0; k < 4; k++) { h = cfma(lc, h, f32x2{acc[i][0][k], acc[i][1][k]}); hr[k] = h[0]; hi[k] = h[1]; }
                sts64(tb + l15 * 168 + q4 * 40 + i * 8, u32x2{pk(hr[0], hr[1]), pk(hr[2], hr[3])});
                sts64(tb + (16 + l15) * 168 + q4 * 40 + i * 8, u32x2{pk(hi[0], hi[1]), pk(hi[2], hi[3])});
            }
            wait_lgkm();
            const unsigned ta = tb + (8 * q4 + ((lane >> 2) & 3)) * 168 + (lane & 3) * 8;
#pragma unroll
            for (int i = 0; i < 5; i++) {
                u32x4 f = ldstr2(ta + i * 32, 4 * 168);
                int rt = 10 * tt + 5 * th + i;
                if (rt < 128) st128(ws, lane * 16, WS_H + (unsigned)(rt * 36 + 4 * g + pg) * 1024u, f);
            }
            wait_vm<0>();
            barrier();
            if (tid == 0) st64(ws, WS_HFLAG + (unsigned)(tt * 9 + g) * 128u, 0, tag);
            STAMP(6);
        } else {
            const int sp = half ? u - 53 : 0;
            if (tid == 0) st64(ws, WS_START + b * 8, 0, tag);
            u32x4 gw[9];
#pragma unroll
            for (int k = 0; k < 9; k++) gw[k] = *(const GAS u32x4*)(P.gw + k * 128 + (lane & 7) * 16);
            for (int rg = sp * 8 + w; rg < 256; rg += 88) {
                const char* xr = P.x + (size_t)(rg * 8 + (lane >> 3)) * 1152 + (lane & 7) * 16;
                u32x4 xv[9];
#pragma unroll
                for (int k = 0; k < 9; k++) xv[k] = *(const GAS u32x4*)(xr + k * 128);
                float s = 0.f;
#pragma unroll
                for (int k = 0; k < 9; k++)
#pragma unroll
                    for (int e = 0; e < 4; e++) s = fmaf(bfhi(xv[k][e]), bfhi(gw[k][e]), fmaf(bflo(xv[k][e]), bflo(gw[k][e]), s));
                s += __shfl_xor(s, 1); s += __shfl_xor(s, 2); s += __shfl_xor(s, 4);
                float gv = 1.f / (1.f + expf(-s));
                if ((lane & 7) == 0) st64(ws, (lane >> 3) * 8, WS_G + (unsigned)rg * 64u, u32x2{__float_as_uint(gv), tag[0]});
            }
#pragma unroll
            for (int e = 0; e < 14; e++) {
                int m = w + 8 * e;
                if (m < 108) { int pr = m / 36; dma_c(P, m - 36 * pr, 3 + 2 * pr, Cc, vrow, sb + pr * STG); }
            }
            if (sp == 0 && w == 0) {
#pragma unroll
                for (int e = 0; e < 4; e++) poll_flags(ws, WS_START + (unsigned)(e * 64 + lane) * 8u, true, tag);
                if (lane == 0) st64(ws, WS_EPOCH, 0, tag);
            }
            wait_vm<0>();
        }
        u32x2 xs[5], ds[5];
        {
            const char* xr = P.x + (size_t)(16 * (rt0 + kh) + l15) * 1152 + Cc * 144 + q4 * 8;
#pragma unroll
            for (int t = 0; t < 5; t++) {
                int o = (t < 4 || q4 < 2) ? t * 32 : 0;
                xs[t] = *(const GAS u32x2*)(xr + o);
                ds[t] = *(const GAS u32x2*)(P.dskip + Cc * 144 + q4 * 8 + o);
            }
        }
        if (w == 0) wait_h(ws, R, 3, 6, lane, tag);
        barrier();
        STAMP(7);
        f32x4 y[2][5];
#pragma unroll
        for (int t = 0; t < 5; t++) { y[0][t] = y[1][t] = f32x4{0, 0, 0, 0}; }
        p2_ssm<12>(ws, y, rt0, 12 * kh, 3 * r4, 3, sb, lane);
        STAMP(8);
        f32x4 res[5];
        khalf_reduce(y, res, sb + S_TR + 2048, w, lane);
#pragma unroll
        for (int t = 0; t < 5; t++) {
            float a0 = fmaf(bflo(xs[t][0]), bflo(ds[t][0]), res[t][0]), a1 = fmaf(bfhi(xs[t][0]), bfhi(ds[t][0]), res[t][1]);
            float a2 = fmaf(bflo(xs[t][1]), bflo(ds[t][1]), res[t][2]), a3 = fmaf(bfhi(xs[t][1]), bfhi(ds[t][1]), res[t][3]);
            st128(ws, lane * 16, WS_YA + (unsigned)((pair * 8 + w) * 5 + t) * 1024u, u32x4{pk(a0, a1), tag[0], pk(a2, a3), tag[0]});
        }
        STAMP(9);
    }
    wait_vm<0>();
    STAMP(15);
    if (lane == 0) {
        u64* d = (u64*)(P.ws + WS_DBG) + (size_t)(b * 8 + w) * 16;
#pragma unroll
        for (int i = 0; i < 16; i++) d[i] = stamp[i];
    }
}

__global__ void k_spin(const u64* st) {
    __shared__ u64 sa[256], se[256];
    int b = threadIdx.x; u64 mn = ~0ull, mx = 0;
    bool isA = (b >> 3) < 16; bool use = AMP_ROLE == 2 || (AMP_ROLE == 0) == isA;
    for (int w = 0; w < AMP_NW; w++) { u64 a = st[(b * AMP_NW + w) * 16], e = st[(b * AMP_NW + w) * 16 + AMP_K]; mn = a < mn ? a : mn; if (use) mx = e > mx ? e : mx; }
    sa[b] = mn; se[b] = mx; __syncthreads();
    if (b == 0) {
        for (int i = 0; i < 256; i++) { mn = sa[i] < mn ? sa[i] : mn; mx = se[i] > mx ? se[i] : mx; }
        u64 span = mx - mn; if (span > 6000) span = 6000;
        u64 t = __builtin_amdgcn_s_memrealtime() + span * 200;
        while (__builtin_amdgcn_s_memrealtime() < t) __builtin_amdgcn_s_sleep(32);
    }
}
extern "C" void kernel_launch(void* const* d_in, const int* in_sizes, int n_in,
                              void* d_out, int out_size, void* d_ws, size_t ws_size,
                              hipStream_t stream) {
    Params P;
    P.x = (const char*)d_in[0];
    P.lre = (const char*)d_in[1];
    P.lim = (const char*)d_in[2];
    P.bre = (const char*)d_in[3];
    P.bim = (const char*)d_in[4];
    P.cre = (const char*)d_in[5];
    P.cim = (const char*)d_in[6];
    P.logdt = (const char*)d_in[7];
    P.dskip = (const char*)d_in[8];
    P.wqkv = (const char*)d_in[10];
    P.wphi = (const char*)d_in[11];
    P.wo = (const char*)d_in[12];
    P.gw = (const char*)d_in[13];
    P.out = (float*)d_out;
    P.ws = (char*)d_ws;
    fused_kernel<<<256, 512, 0, stream>>>(P);
    k_spin<<<1, 256, 0, stream>>>((const u64*)((char*)d_ws + WS_DBG));
}
```

```cpp
#define OUT_AUX 2
#include <hip/hip_runtime.h>
#include <stdint.h>

typedef unsigned short u16;
typedef unsigned long long u64;
typedef short s16x4 __attribute__((ext_vector_type(4)));
typedef __bf16 bf16x2 __attribute__((ext_vector_type(2)));
typedef __bf16 bf16x4 __attribute__((ext_vector_type(4)));
typedef __bf16 bf16x8 __attribute__((ext_vector_type(8)));
typedef float f32x2 __attribute__((ext_vector_type(2)));
typedef float f32x4 __attribute__((ext_vector_type(4)));
typedef unsigned u32x2 __attribute__((ext_vector_type(2)));
typedef unsigned u32x4 __attribute__((ext_vector_type(4)));
#define LAS __attribute__((address_space(3)))
#define GAS __attribute__((address_space(1)))
#define DI __device__ __forceinline__
typedef __amdgpu_buffer_rsrc_t rsrc_t;

#define DBG 0

constexpr unsigned WS_EPOCH = 0;
constexpr unsigned WS_START = 4096;
constexpr unsigned WS_HFLAG = 8192;
constexpr unsigned WS_OFLAG = 24576;
constexpr unsigned WS_G     = 40960;
constexpr unsigned WS_E     = 65536;
constexpr unsigned WS_KV    = 262144;
constexpr unsigned WS_OL    = 1114112;
constexpr unsigned WS_O     = 1376256;
constexpr unsigned WS_H     = WS_O + 128 * 16 * 1024;
constexpr unsigned WS_YA    = WS_H + 128 * 36 * 1024;
constexpr unsigned WS_END   = WS_YA + 128 * 40 * 1024;
constexpr unsigned WS_DBG   = 32u << 20;

constexpr int SPIN_CAP = 1 << 14;
constexpr int AUX_SC1 = 16;

struct Params {
    const char *x, *lre, *lim, *bre, *bim, *cre, *cim, *logdt, *dskip, *wqkv, *wphi, *wo, *gw;
    float* out;
    char* ws;
};

DI float bf2f(unsigned v) { return __uint_as_float(v << 16); }
DI float bflo(unsigned v) { return __uint_as_float(v << 16); }
DI float bfhi(unsigned v) { return __uint_as_float(v & 0xffff0000u); }
DI unsigned pk(float a, float b) { f32x2 t = {a, b}; return __builtin_bit_cast(unsigned, __builtin_convertvector(t, bf16x2)); }
DI f32x4 mfma32(u32x4 a, u32x4 b, f32x4 c) {
    return __builtin_amdgcn_mfma_f32_16x16x32_bf16(__builtin_bit_cast(bf16x8, a), __builtin_bit_cast(bf16x8, b), c, 0, 0, 0);
}
DI f32x4 mfma16(u32x2 a, u32x2 b, f32x4 c) {
    return mfma32(u32x4{a[0], a[1], 0u, 0u}, u32x4{b[0], b[1], 0u, 0u}, c);
}
DI u32x4 lds128(unsigned a) { return *(LAS u32x4*)(size_t)a; }
DI u32x2 lds64(unsigned a) { return *(LAS u32x2*)(size_t)a; }
DI void sts64(unsigned a, u32x2 v) { *(LAS u32x2*)(size_t)a = v; }
DI void sts128(unsigned a, u32x4 v) { *(LAS u32x4*)(size_t)a = v; }
DI u32x2 ldstr(unsigned a) {
    return __builtin_bit_cast(u32x2, __builtin_amdgcn_ds_read_tr16_b64_v4bf16((LAS bf16x4*)(size_t)a));
}
DI u32x4 ldstr2(unsigned a, unsigned d) { u32x2 lo = ldstr(a), hi = ldstr(a + d); return u32x4{lo[0], lo[1], hi[0], hi[1]}; }
DI void dma16(const char* sbase, unsigned voff, unsigned lds) {
    asm volatile("s_mov_b32 m0, %2\n\ts_nop 0\n\tglobal_load_lds_dwordx4 %0, %1" :: "v"(voff), "s"(sbase), "s"(lds) : "memory");
}
template <int N> DI void wait_vm() { asm volatile("s_waitcnt vmcnt(%0)" :: "i"(N) : "memory"); }
DI void wait_lgkm() { asm volatile("s_waitcnt lgkmcnt(0)" ::: "memory"); }
DI void barrier() { asm volatile("s_waitcnt lgkmcnt(0)\n\ts_barrier" ::: "memory"); }
DI int uni(int v) { return __builtin_amdgcn_readfirstlane(v); }
DI u32x4 ld128(rsrc_t r, unsigned v, unsigned s) { return __builtin_amdgcn_raw_buffer_load_b128(r, v, s, AUX_SC1); }
DI u32x2 ld64(rsrc_t r, unsigned v, unsigned s) { return __builtin_amdgcn_raw_buffer_load_b64(r, v, s, AUX_SC1); }
DI void st128(rsrc_t r, unsigned v, unsigned s, u32x4 d) { __builtin_amdgcn_raw_buffer_store_b128(d, r, v, s, AUX_SC1); }
DI void st64(rsrc_t r, unsigned v, unsigned s, u32x2 d) { __builtin_amdgcn_raw_buffer_store_b64(d, r, v, s, AUX_SC1); }
DI void st32(rsrc_t r, unsigned v, unsigned s, unsigned d) { __builtin_amdgcn_raw_buffer_store_b32(d, r, v, s, AUX_SC1); }
DI f32x2 cmul(f32x2 a, f32x2 b) { return f32x2{a[0] * b[0] - a[1] * b[1], a[0] * b[1] + a[1] * b[0]}; }
DI f32x2 cfma(f32x2 a, f32x2 b, f32x2 c) {
    return f32x2{fmaf(a[0], b[0], fmaf(-a[1], b[1], c[0])), fmaf(a[0], b[1], fmaf(a[1], b[0], c[1]))};
}

#define STAMP(i) do {} while (0)
#define CSTART
#define CEND
#define NO_DMA 0
#define NO_CMP 0

DI void poll_flags(rsrc_t ws, unsigned fo, bool need, u32x2 tag) {
#pragma unroll 1
    for (int spin = 0; spin < SPIN_CAP; spin++) {
        asm volatile("" ::: "memory");
        u32x2 f = ld64(ws, fo, 0);
        bool ok = !need || (f[0] == tag[0] && f[1] == tag[1]);
        if (__all(ok)) break;
        __builtin_amdgcn_s_sleep(1);
    }
    asm volatile("" ::: "memory");
}

DI void dma_c(const Params& P, int m, int g0, int Cc, unsigned vrow, unsigned lds) {
    int gi = m >= 18 ? 1 : 0; int mm = m - 18 * gi; int comp = mm >= 9 ? 1 : 0; int n = mm - 9 * comp;
    const char* base = (comp ? P.cim : P.cre) + (size_t)((72 * Cc + 8 * n) * 1152 + (g0 + gi) * 128);
    dma16(base, vrow ^ ((n & 1) << 6), lds + m * 1024);
}

DI void wait_h(rsrc_t ws, int R, int g0, int ng, int lane, u32x2 tag) {
    int t0 = (128 * R) / 160, t1 = (128 * R + 127) / 160;
    int tt = (lane >= 16) ? t1 : t0, g = lane & 15;
    bool need = lane < 32 && g < ng;
    unsigned fo = need ? WS_HFLAG + (unsigned)(tt * 9 + g0 + g) * 128u : WS_HFLAG;
    poll_flags(ws, fo, need, tag);
}

template <int NK>
DI void p2_ssm(rsrc_t ws, f32x4 (&acc)[2][5], int rt0, int k0, int rot, int gfirst, unsigned cbase, int lane) {
    u32x4 hf[NK][2];
#pragma unroll
    for (int i = 0; i < NK; i++) {
        int kk = i + rot; if (kk >= NK) kk -= NK;
        int ks = gfirst * 4 + k0 + kk;
#pragma unroll
        for (int m = 0; m < 2; m++) hf[i][m] = ld128(ws, lane * 16, WS_H + (unsigned)((rt0 + m) * 36 + ks) * 1024u);
    }
    int rho = lane & 15, kq = lane >> 4;
    unsigned lb = (unsigned)((kq >> 1) * 9216 + rho * 128 + (((kq & 1) ^ ((rho >> 1) & 7)) << 4));
#pragma unroll
    for (int i = 0; i < NK; i++) {
        int kk = i + rot; if (kk >= NK) kk -= NK;
        int kl = k0 + kk;
        unsigned a = (lb ^ (unsigned)((kl & 3) << 5)) + cbase + (unsigned)((kl >> 2) * 18432);
#pragma unroll
        for (int t = 0; t < 5; t++) {
            u32x4 w = lds128(a + t * 2048);
#pragma unroll
            for (int m = 0; m < 2; m++) acc[m][t] = mfma32(w, hf[i][m], acc[m][t]);
        }
    }
}

DI void khalf_reduce(f32x4 (&acc)[2][5], f32x4 (&res)[5], unsigned xb, int w, int lane) {
    int kh = w >> 2;
#pragma unroll
    for (int t = 0; t < 5; t++) {
        f32x4 s = kh ? acc[0][t] : acc[1][t];
        sts128(xb + (unsigned)((w * 5 + t) * 1024 + lane * 16), __builtin_bit_cast(u32x4, s));
    }
    barrier();
#pragma unroll
    for (int t = 0; t < 5; t++) {
        f32x4 o = __builtin_bit_cast(f32x4, lds128(xb + (unsigned)(((w ^ 4) * 5 + t) * 1024 + lane * 16)));
        res[t] = (kh ? acc[1][t] : acc[0][t]) + o;
    }
}

DI void feat_map(u32x4 bm, u32x2 bl, unsigned wm, unsigned wl, unsigned (&ph)[9]) {
    float p[20];
#pragma unroll
    for (int u = 0; u < 5; u++) {
        f32x4 c = mfma32(lds128(wm + u * 1024), bm, f32x4{0, 0, 0, 0});
        c = mfma16(lds64(wl + u * 512), bl, c);
        p[4 * u] = c[0]; p[4 * u + 1] = c[1]; p[4 * u + 2] = c[2]; p[4 * u + 3] = c[3];
    }
    float m = p[0];
#pragma unroll
    for (int f = 1; f < 18; f++) m = fmaxf(m, p[f]);
    const float L2E = 1.44269504088896341f;
    float nm = -m * L2E, s = 0.f;
#pragma unroll
    for (int f = 0; f < 18; f++) { p[f] = __builtin_amdgcn_exp2f(fmaf(p[f], L2E, nm)); s += p[f]; }
    float inv = __builtin_amdgcn_rcpf(s);
#pragma unroll
    for (int f = 0; f < 9; f++) ph[f] = pk(p[2 * f] * inv, p[2 * f + 1] * inv);
}

__global__ __launch_bounds__(512, 2) void fused_kernel(Params P) {
    __shared__ __attribute__((aligned(1024))) char smem[163840];
    const unsigned sb = (unsigned)(size_t)(LAS char*)smem;
    STAMP(0);
    const int tid = threadIdx.x, lane = tid & 63, w = uni(tid >> 6);
    const int b = blockIdx.x, xcd = b & 7, r = b >> 3;
    const int r4 = r & 3, rq = (r >> 2) & 3;
    const int Tt = (xcd & 1) * 4 + rq;
    const int R = 2 * Tt + ((xcd >> 1) & 1), Cc = (xcd >> 2) * 4 + r4;
    const int pair = xcd * 16 + (r & 15);
    const rsrc_t ws = __builtin_amdgcn_make_buffer_rsrc(P.ws, 0, WS_END, 0x00020000);
    u64 ep = *(const __attribute__((address_space(4))) u64*)(size_t)(P.ws + WS_EPOCH);
    const int q4 = lane >> 4, l15 = lane & 15;
    const unsigned vrow = (unsigned)((lane >> 3) * 1152 + (((lane & 7) ^ ((lane >> 4) & 3)) << 4));
    const int mq = w & 3, kh = w >> 2;
    const int rt0 = 8 * R + 2 * mq;

    if (r < 16) {
        const int j = (xcd >> 1) * 4 + r4;
        constexpr unsigned STG = 49152, A_WF = 147456, A_WL = A_WF + 5120, A_RAW = A_WF + 10240, A_KVS = A_RAW + 2048;
        constexpr unsigned A_IMG = 82944;
        const int offs = (0x7420 >> (4 * r4)) & 15, offt = (0x7420 >> (4 * rq)) & 15;
        int kc = (offs + offt + (xcd & 1) + (xcd >> 1)) % 9;
        unsigned vw[2];
#pragma unroll
        for (int u = 0; u < 2; u++) {
            int e = 64 * (w + 8 * u) + lane; if (e > 959) e = 959;
            int pos = e / 15, c = e - pos * 15; int part = c / 5, cc = c - part * 5;
            int pp = pos & 15, m = pp >> 1;
            int k = (pos & ~15) + 8 * (m >> 2) + 4 * (pp & 1) + (m & 3);
            vw[u] = (unsigned)(k * 3456 + part * 1152 + ((72 * j) & ~15) + cc * 16);
        }
        const char* xw = P.x + (size_t)(256 * Tt + 32 * w) * 1152;
        auto issue = [&](int kk, unsigned slot) {
            const char* xs = xw + kk * 128;
            const char* wsrc = P.wqkv + (size_t)kk * (64 * 3456);
            dma16(wsrc, vw[0], sb + slot + 32768 + w * 1024);
            if (w < 7) dma16(wsrc, vw[1], sb + slot + 32768 + (w + 8) * 1024);
#pragma unroll
            for (int u = 0; u < 4; u++) dma16(xs + u * 8 * 1152, vrow ^ ((u & 1) << 6), sb + slot + (4 * w + u) * 1024);
        };
        if (w < 2) {
            int e = w * 64 + lane; if (e > 80) e = 80;
            dma16(P.wphi + j * 1296, e * 16, sb + A_RAW + w * 1024);
        }
        issue(kc, 0);
        { int k1 = kc + 1; if (k1 >= 9) k1 -= 9; issue(k1, STG); }
        asm volatile("" : "+s"(ep));
        const u32x2 tag = {(unsigned)(ep + 1), (unsigned)((ep + 1) >> 32)};
        if (tid == 0) st64(ws, WS_START + b * 8, 0, tag);
        unsigned vo[4];
#pragma unroll
        for (int u = 0; u < 4; u++) {
            int n = (u & 1) ? 8 : w;
            int e = 64 * n + lane; int pos = e / 9, c = e - pos * 9;
            int ks = pos >> 5, pp = pos & 15, m = pp >> 1;
            int kq = 2 * ((pos >> 4) & 1) + (m >> 2), je = 4 * (pp & 1) + (m & 3);
            int k = (u < 2) ? 36 * ks + 18 * (kq >> 1) + 8 * (kq & 1) + je
                            : 36 * (8 * ks + 2 * kq + (je >> 2)) + 18 * ((je >> 1) & 1) + 16 + (je & 1);
            vo[u] = (unsigned)(k * 1152 + Cc * 144 + c * 16);
        }
        f32x4 acc[7][2];
#pragma unroll
        for (int i = 0; i < 7; i++) { acc[i][0] = f32x4{0, 0, 0, 0}; acc[i][1] = f32x4{0, 0, 0, 0}; }
        const int tq = (lane >> 2) & 3, tp = lane & 3;
        const unsigned trow = (unsigned)(16 * (q4 >> 1) + 2 * (4 * (q4 & 1) + tq));
        const unsigned ax = sb + (unsigned)((32 * w + l15) * 128 + ((q4 ^ ((lane >> 1) & 7)) << 4));
        const unsigned aw = sb + 32768 + trow * 240 + (j & 1) * 8 + tp * 8;
        const unsigned aw6 = sb + 32768 + trow * 240 + (j & 1) * 8 + 64 + 80 * (tp > 2 ? 2 : tp);
        unsigned slot = 0, nslot = 2 * STG;
        STAMP(1);
#pragma unroll 1
        for (int it = 0; it < 9; it++) {
            wait_vm<5>();
            barrier();
            if (it == 0) { STAMP(2); CSTART; }
            if (it < 7) { int k2 = kc + 2; if (k2 >= 9) k2 -= 9; if (!NO_DMA) issue(k2, nslot); }
            else if (it == 7) {
#pragma unroll
                for (int u = 0; u < 5; u++) dma16(P.wo + u * 72 * 1152, vo[0], sb + (9 * u + w) * 1024);
                if (w < 5) dma16(P.wo + w * 72 * 1152, vo[1], sb + (9 * w + 8) * 1024);
            } else {
#pragma unroll
                for (int u = 5; u < 8; u++) dma16(P.wo + u * 72 * 1152, vo[0], sb + (9 * u + w) * 1024);
                if (w >= 5) dma16(P.wo + w * 72 * 1152, vo[1], sb + (9 * w + 8) * 1024);
                dma16(P.wo, vo[2], sb + (72 + w) * 1024);
                if (w == 0) dma16(P.wo, vo[3], sb + 80 * 1024);
            }
            if (it == 1 && w < 5) {
                int oq = l15 >> 2, hh = oq >> 1, f = 4 * w + (l15 & 3);
                unsigned sg = (oq & 1) ? 0x8000u : 0u;
                unsigned v[12];
#pragma unroll
                for (int e = 0; e < 12; e++) {
                    int col = e < 4 ? 4 * q4 + e : (e < 8 ? 12 + 4 * q4 + e : 24 + e);
                    int hc = col >= 18 ? 1 : 0, d = col - 18 * hc;
                    bool ok = hc == hh && f < 18;
                    unsigned t = *(LAS u16*)(size_t)(sb + A_RAW + (ok ? (hc * 324 + d * 18 + f) * 2 : 0));
                    v[e] = ok ? (t ^ sg) : 0u;
                }
                sts128(sb + A_WF + w * 1024 + lane * 16, u32x4{v[0] | v[1] << 16, v[2] | v[3] << 16, v[4] | v[5] << 16, v[6] | v[7] << 16});
                u32x2 lo = {v[8] | v[9] << 16, v[10] | v[11] << 16}, z = {0u, 0u};
                sts64(sb + A_WL + w * 512 + lane * 8, q4 == 0 ? lo : z);
                sts64(sb + A_WL + 2560 + w * 512 + lane * 8, q4 == 1 ? lo : z);
            }
            if (!NO_CMP)
#pragma unroll
            for (int s = 0; s < 2; s++) {
                u32x4 xf[2];
#pragma unroll
                for (int c = 0; c < 2; c++) xf[c] = lds128((ax ^ (s << 6)) + slot + c * 2048);
#pragma unroll
                for (int i = 0; i < 7; i++) {
                    u32x4 wf = i < 6 ? ldstr2(aw + slot + s * 7680 + (i >> 1) * 80 + (i & 1) * 32, 240)
                                     : ldstr2(aw6 + slot + s * 7680, 240);
#pragma unroll
                    for (int c = 0; c < 2; c++) acc[i][c] = mfma32(wf, xf[c], acc[i][c]);
                }
            }
            kc++; if (kc >= 9) kc -= 9;
            nslot = slot; slot += STG; if (slot == 3 * STG) slot = 0;
        }
        STAMP(3); CEND;
        u32x4 bq[2]; u32x2 bl[2];
        unsigned phk[2][9];
#pragma unroll
        for (int c = 0; c < 2; c++) {
            bq[c] = u32x4{pk(acc[0][c][0], acc[0][c][1]), pk(acc[0][c][2], acc[0][c][3]), pk(acc[1][c][0], acc[1][c][1]), pk(acc[1][c][2], acc[1][c][3])};
            bl[c] = u32x2{pk(acc[6][c][0], acc[6][c][1]), pk(acc[6][c][2], acc[6][c][3])};
            u32x4 bk = {pk(acc[2][c][0], acc[2][c][1]), pk(acc[2][c][2], acc[2][c][3]), pk(acc[3][c][0], acc[3][c][1]), pk(acc[3][c][2], acc[3][c][3])};
            feat_map(bk, bl[c], sb + A_WF + lane * 16, sb + A_WL + 2560 + lane * 8, phk[c]);
        }
        barrier();
#pragma unroll
        for (int c = 0; c < 2; c++) {
            unsigned row = sb + A_IMG + (unsigned)((32 * w + 16 * c + l15) * 240);
#pragma unroll
            for (int m = 0; m < 4; m++) sts64(row + q4 * 40 + m * 8, u32x2{phk[c][2 * m], phk[c][2 * m + 1]});
            sts64(row + q4 * 40 + 32, u32x2{phk[c][8], 0u});
            sts64(row + 160 + q4 * 8, u32x2{pk(acc[4][c][0], acc[4][c][1]), pk(acc[4][c][2], acc[4][c][3])});
            sts64(row + 192 + q4 * 8, u32x2{pk(acc[5][c][0], acc[5][c][1]), pk(acc[5][c][2], acc[5][c][3])});
            if (q4 >= 2) sts64(row + 208 + q4 * 8, q4 == 2 ? bl[c] : u32x2{0x3f80u, 0u});
        }
        if (tid < 32) sts64(sb + A_KVS + 3648 + tid * 8, u32x2{0u, 0u});
        barrier();
        STAMP(4);
        if (w < 6) {
            const int hh = w >= 3 ? 1 : 0, ft = w - 3 * hh;
            const unsigned base = sb + A_IMG + trow * 240;
            const unsigned aa = base + hh * 80 + ft * 32 + ((ft == 2) ? (tp & 1) : tp) * 8;
            const unsigned ab0 = base + 160 + hh * 40 + tp * 8;
            const unsigned ab1 = base + 160 + (tp == 0 ? 32 : 72);
            f32x4 k0 = {0, 0, 0, 0}, k1 = {0, 0, 0, 0};
#pragma unroll
            for (int ks = 0; ks < 8; ks++) {
                u32x4 a = ldstr2(aa + ks * 7680, 240);
                k0 = mfma32(a, ldstr2(ab0 + ks * 7680, 240), k0);
                k1 = mfma32(a, ldstr2(ab1 + ks * 7680, 240), k1);
            }
            const int grp = 4 * ft + q4;
            const unsigned so = WS_KV + (unsigned)(j * 8 + Tt) * 6144u;
            if (grp < 10) {
                st128(ws, (unsigned)(((hh * 19 + l15 + 2 * hh) * 10 + grp) * 16), so, u32x4{pk(k0[0], k0[1]), tag[0], pk(k0[2], k0[3]), tag[0]});
                int d1 = l15 == 4 ? 18 : (hh ? l15 - 2 : l15 + 16);
                bool ok = l15 == 4 || (hh ? (l15 == 2 || l15 == 3) : l15 < 2);
                if (ok) st128(ws, (unsigned)(((hh * 19 + d1) * 10 + grp) * 16), so, u32x4{pk(k1[0], k1[1]), tag[0], pk(k1[2], k1[3]), tag[0]});
            }
        }
        STAMP(5);
        unsigned phq[2][9];
#pragma unroll
        for (int c = 0; c < 2; c++) feat_map(bq[c], bl[c], sb + A_WF + lane * 16, sb + A_WL + lane * 8, phq[c]);
        barrier();
#pragma unroll
        for (int u = 0; u < 7; u++) {
            int m = w + 8 * u;
            if (m < 36) dma_c(P, m, 0, Cc, vrow, sb + A_IMG);
            else if (m < 54) dma_c(P, m - 36, 2, Cc, vrow, sb + A_IMG + 36864);
        }
        STAMP(6);
        if (w < 6) {
            const bool act = tid < 380;
            const unsigned vo_ = act ? tid * 16 : 0;
            u32x4 g[8];
#pragma unroll 1
            for (int spin = 0; spin < SPIN_CAP; spin++) {
                asm volatile("" ::: "memory");
                bool ok = true;
#pragma unroll
                for (int t = 0; t < 8; t++) g[t] = ld128(ws, vo_, WS_KV + (unsigned)(j * 8 + t) * 6144u);
#pragma unroll
                for (int t = 0; t < 8; t++) ok = ok && g[t][1] == tag[0] && g[t][3] == tag[0];
                if (__all(ok)) break;
                __builtin_amdgcn_s_sleep(1);
            }
            float s0 = 0, s1 = 0, s2 = 0, s3 = 0;
#pragma unroll
            for (int t = 0; t < 8; t++) { s0 += bflo(g[t][0]); s1 += bfhi(g[t][0]); s2 += bflo(g[t][2]); s3 += bfhi(g[t][2]); }
            int row = tid / 5, c5 = tid - row * 5;
            if (act) sts64(sb + A_KVS + row * 48 + c5 * 8, u32x2{pk(s0, s1), pk(s2, s3)});
        }
        barrier();
        STAMP(7);
        {
            const int oq = l15 >> 2, hh = oq >> 1;
            const bool on = (q4 >> 1) == hh;
            f32x4 nm[2][3];
#pragma unroll
            for (int u = 0; u < 3; u++) {
                int sl = l15 & 3;
                int d = u < 2 ? 8 * (oq & 1) + 4 * u + sl : (sl < 3 ? 16 + sl : 18);
                unsigned a = sb + A_KVS + (on ? (unsigned)(((hh * 19 + d) * 2 + (q4 & 1)) * 48) : 3648u);
                u32x4 a0 = lds128(a), a1 = lds128(a + 16); u32x2 a2 = lds64(a + 32);
#pragma unroll
                for (int c = 0; c < 2; c++) {
                    f32x4 t = mfma32(a0, u32x4{phq[c][0], phq[c][1], phq[c][2], phq[c][3]}, f32x4{0, 0, 0, 0});
                    t = mfma32(a1, u32x4{phq[c][4], phq[c][5], phq[c][6], phq[c][7]}, t);
                    nm[c][u] = mfma16(a2, u32x2{phq[c][8], 0u}, t);
                }
            }
#pragma unroll
            for (int c = 0; c < 2; c++) {
                float inv = __builtin_amdgcn_rcpf(nm[c][2][2] + 1e-6f);
                unsigned rt = (unsigned)(16 * Tt + 2 * w + c);
                st128(ws, lane * 16, WS_O + (rt * 16 + j) * 1024u,
                      u32x4{pk(nm[c][0][0] * inv, nm[c][0][1] * inv), pk(nm[c][0][2] * inv, nm[c][0][3] * inv),
                            pk(nm[c][1][0] * inv, nm[c][1][1] * inv), pk(nm[c][1][2] * inv, nm[c][1][3] * inv)});
                if (!(q4 & 1)) st32(ws, l15 * 8 + (q4 >> 1) * 4, WS_OL + (rt * 16 + j) * 128u, pk(nm[c][2][0] * inv, nm[c][2][1] * inv));
            }
        }
        wait_vm<0>();
        if (w == 1) wait_h(ws, R, 0, 3, lane, tag);
        barrier();
        if (tid == 0) st64(ws, WS_OFLAG + (unsigned)(Tt * 16 + j) * 128u, 0, tag);
        STAMP(8);
        f32x4 ya[2][5], yb[2][5];
#pragma unroll
        for (int t = 0; t < 5; t++) { ya[0][t] = ya[1][t] = yb[0][t] = yb[1][t] = f32x4{0, 0, 0, 0}; }
        p2_ssm<6>(ws, ya, rt0, 6 * kh, (r4 * 3) >> 1, 0, sb + A_IMG, lane);
        STAMP(9);
        if (w == 0) poll_flags(ws, WS_OFLAG + (unsigned)(Tt * 16 + l15) * 128u, true, tag);
        barrier();
        STAMP(10);
        {
            u32x4 of[9][2];
            const int rot = 2 * r4;
#pragma unroll
            for (int i = 0; i < 8; i++) {
                int ks = 8 * kh + ((i + rot) & 7);
#pragma unroll
                for (int m = 0; m < 2; m++) of[i][m] = ld128(ws, lane * 16, WS_O + (unsigned)((rt0 + m) * 16 + ks) * 1024u);
            }
#pragma unroll
            for (int m = 0; m < 2; m++) {
                unsigned so = WS_OL + (unsigned)((rt0 + m) * 16 + 8 * kh) * 128u;
                u32x2 lo = ld64(ws, q4 * 256 + l15 * 8, so), hi = ld64(ws, q4 * 256 + 128 + l15 * 8, so);
                of[8][m] = u32x4{lo[0], lo[1], hi[0], hi[1]};
            }
            const unsigned ab = sb + trow * 144 + tp * 8;
#pragma unroll
            for (int i = 0; i < 9; i++) {
                int ks = i < 8 ? 8 * kh + ((i + rot) & 7) : 16 + kh;
                unsigned a = ab + (unsigned)(ks * 4608);
#pragma unroll
                for (int t = 0; t < 5; t++) {
                    u32x4 wf = ldstr2(a + t * 32, 144);
#pragma unroll
                    for (int m = 0; m < 2; m++) yb[m][t] = mfma32(wf, of[i][m], yb[m][t]);
                }
            }
        }
        STAMP(11);
        float gt[2];
        u32x4 yv[5];
#pragma unroll 1
        for (int spin = 0; spin < SPIN_CAP; spin++) {
            asm volatile("" ::: "memory");
            u32x2 g0 = ld64(ws, l15 * 8, WS_G + (unsigned)rt0 * 128u), g1 = ld64(ws, l15 * 8 + 128, WS_G + (unsigned)rt0 * 128u);
#pragma unroll
            for (int t = 0; t < 5; t++) yv[t] = ld128(ws, lane * 16, WS_YA + (unsigned)((pair * 8 + w) * 5 + t) * 1024u);
            bool ok = g0[1] == tag[0] && g1[1] == tag[0];
#pragma unroll
            for (int t = 0; t < 5; t++) ok = ok && yv[t][1] == tag[0] && yv[t][3] == tag[0];
            gt[0] = __uint_as_float(g0[0]); gt[1] = __uint_as_float(g1[0]);
            if (__all(ok)) break;
            __builtin_amdgcn_s_sleep(1);
        }
        STAMP(12);
#pragma unroll
        for (int m = 0; m < 2; m++)
#pragma unroll
            for (int t = 0; t < 5; t++) yb[m][t] = gt[m] * ya[m][t] + (1.f - gt[m]) * yb[m][t];
        barrier();
        f32x4 res[5];
        khalf_reduce(yb, res, sb, w, lane);
        const float g = kh ? gt[1] : gt[0];
        const rsrc_t ors = __builtin_amdgcn_make_buffer_rsrc(P.out, 0, 2048 * 576 * 4, 0x00020000);
        float* orow = P.out + (size_t)(16 * (rt0 + kh) + l15) * 576 + 72 * Cc + 4 * q4;
#pragma unroll
        for (int t = 0; t < 5; t++) {
            f32x4 o = res[t] + g * f32x4{bflo(yv[t][0]), bfhi(yv[t][0]), bflo(yv[t][2]), bfhi(yv[t][2])};
            if (t < 4 || q4 < 2) __builtin_amdgcn_raw_buffer_store_b128(__builtin_bit_cast(u32x4, o), ors, (unsigned)((char*)(orow + 16 * t) - (char*)P.out), 0, OUT_AUX);
        }
        STAMP(13);
    } else {
        constexpr unsigned STG = 36864, S_TR = 110592, S_AGG = S_TR + 8 * 5376;
        const int half = xcd & 1, u = (xcd >> 1) * 16 + (r - 16);
        const int nt = half ? 6 : 7;
        const int g = half ? u / 6 : u / 7, tt = u - g * nt + (half ? 7 : 0);
        asm volatile("" : "+s"(ep));
        const u32x2 tag = {(unsigned)(ep + 1), (unsigned)((ep + 1) >> 32)};
        if (g < 9) {
            const int th = w >> 2, pg = w & 3;
            int kc = (tt * 4 + g * 2 + 1) % 9;
            unsigned vx[3];
#pragma unroll
            for (int e = 0; e < 3; e++) {
                int n = w + 8 * e; int tile = n >> 1; int h2 = tile >= 5 ? 1 : 0, i = tile - 5 * h2;
                int t = 160 * tt + 80 * h2 + 4 * i + 40 * (n & 1) + 20 * (lane >> 5) + ((lane >> 3) & 3);
                if (t > 2047) t = 2047;
                vx[e] = (unsigned)(t * 1152 + (((lane & 7) ^ ((lane >> 4) & 3) ^ ((n & 1) << 2)) << 4));
            }
            const char* bw = (((w >> 1) & 1) ? P.bim : P.bre) + (size_t)(64 * g + 16 * (w >> 2) + 8 * (w & 1)) * 1152;
            const unsigned vb = vrow ^ ((w & 1) << 6);
            auto issue = [&](int kk, unsigned slot) {
                const char* xs = P.x + kk * 128;
                dma16(bw + kk * 128, vb, sb + slot + 20480 + w * 1024);
                dma16(bw + kk * 128 + 32 * 1152, vb, sb + slot + 20480 + (w + 8) * 1024);
                dma16(xs, vx[0], sb + slot + w * 1024);
                dma16(xs, vx[1], sb + slot + (w + 8) * 1024);
                if (w < 4) dma16(xs, vx[2], sb + slot + (w + 16) * 1024);
            };
            const int p = 64 * g + 16 * pg + l15;
            unsigned r_a = *(const GAS u16*)(P.lre + p * 2), r_b = *(const GAS u16*)(P.lim + p * 2), r_d = *(const GAS u16*)(P.logdt + p * 2);
            issue(kc, 0);
            { int k1 = kc + 1; if (k1 >= 9) k1 -= 9; issue(k1, STG); }
            if (tid == 0) st64(ws, WS_START + b * 8, 0, tag);
            f32x4 acc[5][2];
#pragma unroll
            for (int i = 0; i < 5; i++) { acc[i][0] = f32x4{0, 0, 0, 0}; acc[i][1] = f32x4{0, 0, 0, 0}; }
            const unsigned sw = (unsigned)(l15 * 128 + ((q4 ^ ((lane >> 1) & 7)) << 4));
            const unsigned ax = sb + th * 10240 + sw, ab = sb + 20480 + pg * 4096 + sw;
            unsigned slot = 0, nslot = 2 * STG;
            STAMP(1);
#pragma unroll 1
            for (int it = 0; it < 9; it++) {
                wait_vm<4>();
                barrier();
                if (it == 0) { STAMP(2); CSTART; }
                if (it < 7) { int k2 = kc + 2; if (k2 >= 9) k2 -= 9; if (!NO_DMA) issue(k2, nslot); }
                else {
#pragma unroll
                    for (int e = 0; e < 5; e++) { int m = w + 8 * e; if (m < 36) dma_c(P, m, 3 + 2 * (it - 7), Cc, vrow, sb + nslot); }
                }
                if (!NO_CMP)
#pragma unroll
                for (int s = 0; s < 2; s++) {
                    u32x4 bf[2];
#pragma unroll
                    for (int c = 0; c < 2; c++) bf[c] = lds128((ab ^ (s << 6)) + slot + c * 2048);
#pragma unroll
                    for (int i = 0; i < 5; i++) {
                        u32x4 xf = lds128((ax ^ (s << 6)) + slot + i * 2048);
#pragma unroll
                        for (int c = 0; c < 2; c++) acc[i][c] = mfma32(xf, bf[c], acc[i][c]);
                    }
                }
                kc++; if (kc >= 9) kc -= 9;
                nslot = slot; slot += STG; if (slot == 3 * STG) slot = 0;
            }
            STAMP(3); CEND;
            f32x2 lam, coef;
            {
                float a = bf2f(r_a), bb = bf2f(r_b);
                float dt = bflo(pk(expf(bf2f(r_d)), 0.f));
                float e = expf(dt * a), sn, cs;
                sincosf(dt * bb, &sn, &cs);
                lam = f32x2{e * cs, e * sn};
                float nr = lam[0] - 1.f, ni = lam[1], inv = 2.f / (a * a + bb * bb);
                coef = f32x2{(nr * a + ni * bb) * inv, (ni * a - nr * bb) * inv};
            }
            f32x2 e = {0.f, 0.f};
#pragma unroll
            for (int i = 0; i < 5; i++)
#pragma unroll
                for (int k = 0; k < 4; k++) e = cfma(lam, e, f32x2{acc[i][0][k], acc[i][1][k]});
            const int seg = th * 4 + q4;
            sts64(sb + S_AGG + seg * 512 + (pg * 16 + l15) * 8, __builtin_bit_cast(u32x2, e));
            f32x2 l2 = cmul(lam, lam), l4 = cmul(l2, l2), l5 = cmul(l4, lam), l10 = cmul(l5, l5), l20 = cmul(l10, l10);
            barrier();
#pragma unroll
            for (int e2 = 0; e2 < 5; e2++) { int m = w + 8 * e2; if (m < 36) dma_c(P, m, 7, Cc, vrow, sb + 2 * STG); }
            f32x2 pre = {0.f, 0.f}, pw = {1.f, 0.f}, run = {0.f, 0.f}, rpw = {1.f, 0.f};
#pragma unroll
            for (int s = 0; s < 8; s++) {
                if (s == seg) { pre = run; pw = rpw; }
                run = cfma(l20, run, __builtin_bit_cast(f32x2, lds64(sb + S_AGG + s * 512 + (pg * 16 + l15) * 8)));
                rpw = cmul(rpw, l20);
            }
            if (th == 0 && q4 == 0)
                st128(ws, (pg * 16 + l15) * 16, WS_E + (unsigned)(tt * 9 + g) * 1024u,
                      u32x4{__float_as_uint(run[0]), tag[0], __float_as_uint(run[1]), tag[0]});
            STAMP(4);
            const f32x2 l160 = rpw, l320 = cmul(l160, l160), l640 = cmul(l320, l320);
            f32x2 wq = (q4 & 1) ? l160 : f32x2{1.f, 0.f};
            if (q4 & 2) wq = cmul(wq, l320);
#pragma unroll
            for (int i = 0; i < 5; i++)
#pragma unroll
                for (int k = 0; k < 4; k++) {
                    float ur = acc[i][0][k], ui = acc[i][1][k];
                    acc[i][0][k] = coef[0] * ur - coef[1] * ui;
                    acc[i][1][k] = -coef[0] * ui - coef[1] * ur;
                }
            f32x2 ev[3];
#pragma unroll 1
            for (int spin = 0; spin < SPIN_CAP; spin++) {
                asm volatile("" ::: "memory");
                bool ok = true;
#pragma unroll
                for (int m = 0; m < 3; m++) {
                    int t2 = tt - 1 - q4 - 4 * m;
                    bool need = t2 >= 0;
                    u32x4 v = ld128(ws, (unsigned)((need ? t2 : 0) * 9216 + (pg * 16 + l15) * 16), WS_E + (unsigned)g * 1024u);
                    ok = ok && (!need || (v[1] == tag[0] && v[3] == tag[0]));
                    ev[m] = need ? f32x2{__uint_as_float(v[0]), __uint_as_float(v[2])} : f32x2{0.f, 0.f};
                }
                if (__all(ok)) break;
                __builtin_amdgcn_s_sleep(1);
            }
            STAMP(5);
            f32x2 cr = cmul(wq, cfma(l640, cfma(l640, ev[2], ev[1]), ev[0]));
            cr[0] += __shfl_xor(cr[0], 16); cr[1] += __shfl_xor(cr[1], 16);
            cr[0] += __shfl_xor(cr[0], 32); cr[1] += __shfl_xor(cr[1], 32);
            f32x2 h = cmul(coef, cfma(pw, cr, pre));
            h[1] = -h[1];
            const f32x2 lc = {lam[0], -lam[1]};
            const unsigned tb = sb + S_TR + w * 5376;
#pragma unroll
            for (int i = 0; i < 5; i++) {
                float hr[4], hi[4];
#pragma unroll
                for (int k = 0; k < 4; k++) { h = cfma(lc, h, f32x2{acc[i][0][k], acc[i][1][k]}); hr[k] = h[0]; hi[k] = h[1]; }
                sts64(tb + l15 * 168 + q4 * 40 + i * 8, u32x2{pk(hr[0], hr[1]), pk(hr[2], hr[3])});
                sts64(tb + (16 + l15) * 168 + q4 * 40 + i * 8, u32x2{pk(hi[0], hi[1]), pk(hi[2], hi[3])});
            }
            wait_lgkm();
            const unsigned ta = tb + (8 * q4 + ((lane >> 2) & 3)) * 168 + (lane & 3) * 8;
#pragma unroll
            for (int i = 0; i < 5; i++) {
                u32x4 f = ldstr2(ta + i * 32, 4 * 168);
                int rt = 10 * tt + 5 * th + i;
                if (rt < 128) st128(ws, lane * 16, WS_H + (unsigned)(rt * 36 + 4 * g + pg) * 1024u, f);
            }
            wait_vm<0>();
            barrier();
            if (tid == 0) st64(ws, WS_HFLAG + (unsigned)(tt * 9 + g) * 128u, 0, tag);
            STAMP(6);
        } else {
            const int sp = half ? u - 53 : 0;
            if (tid == 0) st64(ws, WS_START + b * 8, 0, tag);
            u32x4 gw[9];
#pragma unroll
            for (int k = 0; k < 9; k++) gw[k] = *(const GAS u32x4*)(P.gw + k * 128 + (lane & 7) * 16);
            for (int rg = sp * 8 + w; rg < 256; rg += 88) {
                const char* xr = P.x + (size_t)(rg * 8 + (lane >> 3)) * 1152 + (lane & 7) * 16;
                u32x4 xv[9];
#pragma unroll
                for (int k = 0; k < 9; k++) xv[k] = *(const GAS u32x4*)(xr + k * 128);
                float s = 0.f;
#pragma unroll
                for (int k = 0; k < 9; k++)
#pragma unroll
                    for (int e = 0; e < 4; e++) s = fmaf(bfhi(xv[k][e]), bfhi(gw[k][e]), fmaf(bflo(xv[k][e]), bflo(gw[k][e]), s));
                s += __shfl_xor(s, 1); s += __shfl_xor(s, 2); s += __shfl_xor(s, 4);
                float gv = 1.f / (1.f + expf(-s));
                if ((lane & 7) == 0) st64(ws, (lane >> 3) * 8, WS_G + (unsigned)rg * 64u, u32x2{__float_as_uint(gv), tag[0]});
            }
#pragma unroll
            for (int e = 0; e < 14; e++) {
                int m = w + 8 * e;
                if (m < 108) { int pr = m / 36; dma_c(P, m - 36 * pr, 3 + 2 * pr, Cc, vrow, sb + pr * STG); }
            }
            if (sp == 0 && w == 0) {
#pragma unroll
                for (int e = 0; e < 4; e++) poll_flags(ws, WS_START + (unsigned)(e * 64 + lane) * 8u, true, tag);
                if (lane == 0) st64(ws, WS_EPOCH, 0, tag);
            }
            wait_vm<0>();
        }
        u32x2 xs[5], ds[5];
        {
            const char* xr = P.x + (size_t)(16 * (rt0 + kh) + l15) * 1152 + Cc * 144 + q4 * 8;
#pragma unroll
            for (int t = 0; t < 5; t++) {
                int o = (t < 4 || q4 < 2) ? t * 32 : 0;
                xs[t] = *(const GAS u32x2*)(xr + o);
                ds[t] = *(const GAS u32x2*)(P.dskip + Cc * 144 + q4 * 8 + o);
            }
        }
        if (w == 0) wait_h(ws, R, 3, 6, lane, tag);
        barrier();
        STAMP(7);
        f32x4 y[2][5];
#pragma unroll
        for (int t = 0; t < 5; t++) { y[0][t] = y[1][t] = f32x4{0, 0, 0, 0}; }
        p2_ssm<12>(ws, y, rt0, 12 * kh, 3 * r4, 3, sb, lane);
        STAMP(8);
        f32x4 res[5];
        khalf_reduce(y, res, sb + S_TR + 2048, w, lane);
#pragma unroll
        for (int t = 0; t < 5; t++) {
            float a0 = fmaf(bflo(xs[t][0]), bflo(ds[t][0]), res[t][0]), a1 = fmaf(bfhi(xs[t][0]), bfhi(ds[t][0]), res[t][1]);
            float a2 = fmaf(bflo(xs[t][1]), bflo(ds[t][1]), res[t][2]), a3 = fmaf(bfhi(xs[t][1]), bfhi(ds[t][1]), res[t][3]);
            st128(ws, lane * 16, WS_YA + (unsigned)((pair * 8 + w) * 5 + t) * 1024u, u32x4{pk(a0, a1), tag[0], pk(a2, a3), tag[0]});
        }
        STAMP(9);
    }
}

extern "C" void kernel_launch(void* const* d_in, const int* in_sizes, int n_in,
                              void* d_out, int out_size, void* d_ws, size_t ws_size,
                              hipStream_t stream) {
    Params P;
    P.x = (const char*)d_in[0];
    P.lre = (const char*)d_in[1];
    P.lim = (const char*)d_in[2];
    P.bre = (const char*)d_in[3];
    P.bim = (const char*)d_in[4];
    P.cre = (const char*)d_in[5];
    P.cim = (const char*)d_in[6];
    P.logdt = (const char*)d_in[7];
    P.dskip = (const char*)d_in[8];
    P.wqkv = (const char*)d_in[10];
    P.wphi = (const char*)d_in[11];
    P.wo = (const char*)d_in[12];
    P.gw = (const char*)d_in[13];
    P.out = (float*)d_out;
    P.ws = (char*)d_ws;
    fused_kernel<<<256, 512, 0, stream>>>(P);
}
```

```cpp
#define SPDELAY 80
#include <hip/hip_runtime.h>
#include <stdint.h>

typedef unsigned short u16;
typedef unsigned long long u64;
typedef short s16x4 __attribute__((ext_vector_type(4)));
typedef __bf16 bf16x2 __attribute__((ext_vector_type(2)));
typedef __bf16 bf16x4 __attribute__((ext_vector_type(4)));
typedef __bf16 bf16x8 __attribute__((ext_vector_type(8)));
typedef float f32x2 __attribute__((ext_vector_type(2)));
typedef float f32x4 __attribute__((ext_vector_type(4)));
typedef unsigned u32x2 __attribute__((ext_vector_type(2)));
typedef unsigned u32x4 __attribute__((ext_vector_type(4)));
#define LAS __attribute__((address_space(3)))
#define GAS __attribute__((address_space(1)))
#define DI __device__ __forceinline__
typedef __amdgpu_buffer_rsrc_t rsrc_t;

#define DBG 0

constexpr unsigned WS_EPOCH = 0;
constexpr unsigned WS_START = 4096;
constexpr unsigned WS_HFLAG = 24576;
constexpr unsigned WS_OFLAG = WS_HFLAG + 16384;
constexpr unsigned WS_KVH   = WS_OFLAG + 16384;
constexpr unsigned WS_YAF   = WS_KVH + 16384;
constexpr unsigned WS_E     = WS_YAF + 65536;
static_assert(WS_E + 117 * 1024 <= 262144 && WS_HFLAG + 29 * 8192 <= 262144, "workspace map");
constexpr unsigned WS_KV    = 262144;
constexpr unsigned WS_OL    = 1245184;
constexpr unsigned WS_O     = 1507328;
constexpr unsigned WS_H     = WS_O + 128 * 16 * 1024;
constexpr unsigned WS_YA    = WS_H + 128 * 36 * 1024;
constexpr unsigned WS_END   = WS_YA + 128 * 20 * 1024;
constexpr unsigned WS_DBG   = 32u << 20;

constexpr int SPIN_CAP = 1 << 14;
constexpr int AUX_SC1 = 16;

struct Params {
    const char *x, *lre, *lim, *bre, *bim, *cre, *cim, *logdt, *dskip, *wqkv, *wphi, *wo, *gw;
    float* out;
    char* ws;
};

DI float bf2f(unsigned v) { return __uint_as_float(v << 16); }
DI float bflo(unsigned v) { return __uint_as_float(v << 16); }
DI float bfhi(unsigned v) { return __uint_as_float(v & 0xffff0000u); }
DI unsigned pk(float a, float b) { f32x2 t = {a, b}; return __builtin_bit_cast(unsigned, __builtin_convertvector(t, bf16x2)); }
DI f32x4 mfma32(u32x4 a, u32x4 b, f32x4 c) {
    return __builtin_amdgcn_mfma_f32_16x16x32_bf16(__builtin_bit_cast(bf16x8, a), __builtin_bit_cast(bf16x8, b), c, 0, 0, 0);
}
DI f32x4 mfma16(u32x2 a, u32x2 b, f32x4 c) {
    return mfma32(u32x4{a[0], a[1], 0u, 0u}, u32x4{b[0], b[1], 0u, 0u}, c);
}
DI u32x4 lds128(unsigned a) { return *(LAS u32x4*)(size_t)a; }
DI u32x2 lds64(unsigned a) { return *(LAS u32x2*)(size_t)a; }
DI void sts64(unsigned a, u32x2 v) { *(LAS u32x2*)(size_t)a = v; }
DI void sts128(unsigned a, u32x4 v) { *(LAS u32x4*)(size_t)a = v; }
DI u32x2 ldstr(unsigned a) {
    return __builtin_bit_cast(u32x2, __builtin_amdgcn_ds_read_tr16_b64_v4bf16((LAS bf16x4*)(size_t)a));
}
DI u32x4 ldstr2(unsigned a, unsigned d) { u32x2 lo = ldstr(a), hi = ldstr(a + d); return u32x4{lo[0], lo[1], hi[0], hi[1]}; }
DI void dma16(const char* sbase, unsigned voff, unsigned lds) {
    asm volatile("s_mov_b32 m0, %2\n\ts_nop 0\n\tglobal_load_lds_dwordx4 %0, %1" :: "v"(voff), "s"(sbase), "s"(lds) : "memory");
}
template <int N> DI void wait_vm() { asm volatile("s_waitcnt vmcnt(%0)" :: "i"(N) : "memory"); }
DI void wait_lgkm() { asm volatile("s_waitcnt lgkmcnt(0)" ::: "memory"); }
DI void barrier() { asm volatile("s_waitcnt lgkmcnt(0)\n\ts_barrier" ::: "memory"); }
DI int uni(int v) { return __builtin_amdgcn_readfirstlane(v); }
DI u32x4 ld128(rsrc_t r, unsigned v, unsigned s) { return __builtin_amdgcn_raw_buffer_load_b128(r, v, s, AUX_SC1); }
DI unsigned ld32(rsrc_t r, unsigned v, unsigned s) { return __builtin_amdgcn_raw_buffer_load_b32(r, v, s, AUX_SC1); }
DI u32x2 ld64(rsrc_t r, unsigned v, unsigned s) { return __builtin_amdgcn_raw_buffer_load_b64(r, v, s, AUX_SC1); }
DI void st128(rsrc_t r, unsigned v, unsigned s, u32x4 d) { __builtin_amdgcn_raw_buffer_store_b128(d, r, v + s, 0, AUX_SC1); }
DI void st64(rsrc_t r, unsigned v, unsigned s, u32x2 d) { __builtin_amdgcn_raw_buffer_store_b64(d, r, v, s, AUX_SC1); }
DI void st32(rsrc_t r, unsigned v, unsigned s, unsigned d) { __builtin_amdgcn_raw_buffer_store_b32(d, r, v, s, AUX_SC1); }
DI f32x2 cmul(f32x2 a, f32x2 b) { return f32x2{a[0] * b[0] - a[1] * b[1], a[0] * b[1] + a[1] * b[0]}; }
DI f32x2 cfma(f32x2 a, f32x2 b, f32x2 c) {
    return f32x2{fmaf(a[0], b[0], fmaf(-a[1], b[1], c[0])), fmaf(a[0], b[1], fmaf(a[1], b[0], c[1]))};
}

#define STAMP(i) do {} while (0)
#define ITSTAMP(i)
#define CSTART
#define CEND

DI void poll_flags(rsrc_t ws, unsigned fo, bool need, u32x2 tag) {
#pragma unroll 1
    for (int spin = 0; spin < SPIN_CAP; spin++) {
        asm volatile("" ::: "memory");
        u32x2 f = ld64(ws, fo, 0);
        bool ok = !need || (f[0] == tag[0] && f[1] == tag[1]);
        if (__all(ok)) break;
        __builtin_amdgcn_s_sleep(1);
    }
    asm volatile("" ::: "memory");
}

template <int N, int PACE> DI void dma_cimg(const Params& P, int grp, int comp, int Cc, unsigned vrow, unsigned lds) {
    const char* base = (comp ? P.cim : P.cre) + (size_t)(72 * Cc * 1152 + grp * 128);
#pragma unroll
    for (int n = 0; n < N; n++) { dma16(base + n * 8 * 1152, vrow ^ ((n & 1) << 6), lds + n * 1024); if (PACE) __builtin_amdgcn_s_sleep(PACE); }
}
DI void touch64(const char* src, unsigned voff, unsigned lds) {
    asm volatile("s_mov_b32 m0, %2\n\ts_nop 0\n\tglobal_load_lds_dword %0, %1" :: "v"(voff), "s"(src), "s"(lds) : "memory");
}

#define SCHED_ALL() __builtin_amdgcn_sched_barrier(0)
template <int NM, int ND, int PER> DI void interleave() {
#pragma unroll
    for (int i = 0; i < (ND + PER - 1) / PER; i++) {
        __builtin_amdgcn_sched_group_barrier(0x8, 1, 0);
        __builtin_amdgcn_sched_group_barrier(0x100, PER, 0);
    }
    __builtin_amdgcn_sched_group_barrier(0x8, NM, 0);
}
template <int V> DI void wait_both() { __builtin_amdgcn_s_waitcnt((V & 15) | ((V >> 4) << 14) | (7 << 4)); }

DI f32x2 pfma(f32x2 a, f32x2 b, f32x2 c) { return __builtin_elementwise_fma(a, b, c); }
DI f32x2 lo2(f32x4 v) { return f32x2{v[0], v[1]}; }
DI f32x2 hi2(f32x4 v) { return f32x2{v[2], v[3]}; }
DI void swap32(float x, float& lo, float& hi) { auto r = __builtin_amdgcn_permlane32_swap(__float_as_uint(x), __float_as_uint(x), false, false); lo = __uint_as_float(r[0]); hi = __uint_as_float(r[1]); }
DI void swap16(float x, float& lo, float& hi) { auto r = __builtin_amdgcn_permlane16_swap(__float_as_uint(x), __float_as_uint(x), false, false); lo = __uint_as_float(r[0]); hi = __uint_as_float(r[1]); }
DI void gather4(float x, float (&o)[4]) { float a, b; swap32(x, a, b); swap16(a, o[0], o[1]); swap16(b, o[2], o[3]); }
DI float sum4(float x) { float a, b; swap32(x, a, b); a += b; swap16(a, a, b); return a + b; }
DI unsigned tagdiff(u32x4 v, unsigned tag) { return __builtin_amdgcn_bitop3_b32(v[1], v[3], tag, 0x7e); }
DI u32x4 sel_frag(int l15, int q4) {
    unsigned v = (l15 >> 2) == q4 ? ((l15 & 1) ? 0x3f800000u : 0x3f80u) : 0u;
    return u32x4{(l15 & 2) ? 0u : v, 0u, (l15 & 2) ? v : 0u, 0u};
}

constexpr unsigned KVB = 6400;

template <class RD, class LD>
DI void p2_step(f32x4 (&y)[2][5], const u32x4 (&wc)[5], const u32x4 (&ac)[2], RD rd, LD ld) {
#pragma unroll
    for (int n = 0; n < 10; n++) {
        asm volatile("v_mfma_f32_16x16x32_bf16 %0, %1, %2, %0" : "+v"(y[n & 1][n >> 1]) : "v"(wc[n >> 1]), "v"(ac[n & 1]));
        SCHED_ALL();
        if (n < 5) rd(n); else if (n < 7) ld(n - 5);
        SCHED_ALL();
    }
    asm volatile("" :: "v"(wc[0]), "v"(wc[1]), "v"(wc[2]), "v"(wc[3]), "v"(wc[4]), "v"(ac[0]), "v"(ac[1]));
}
DI void mfma_drain() { SCHED_ALL(); asm volatile("s_nop 7\n\ts_nop 1"); SCHED_ALL(); }

DI void feat_mm(u32x4 bm, u32x4 bl, const u32x4 (&am)[5], const u32x4 (&al)[5], f32x4 (&t)[5]) {
#pragma unroll
    for (int u = 0; u < 5; u++) t[u] = mfma32(al[u], bl, mfma32(am[u], bm, f32x4{0, 0, 0, 0}));
}
DI float max3(float a, float b, float c) { return __builtin_fmaxf(__builtin_fmaxf(a, b), c); }
DI void feat_sm(const f32x4 (&t)[5], unsigned (&ph)[9]) {
    float m0 = max3(max3(max3(t[0][0], t[0][1], t[0][2]), t[0][3], t[1][0]), t[1][1], t[1][2]);
    float m1 = max3(max3(max3(t[2][0], t[2][1], t[2][2]), t[2][3], t[3][0]), t[3][1], t[3][2]);
    float m = max3(max3(m0, t[1][3], t[3][3]), m1, max3(t[4][0], t[4][1], t[0][0]));
    const float L2E = 1.44269504088896341f;
    const float nm = -m * L2E;
    f32x2 e[9];
#pragma unroll
    for (int f = 0; f < 9; f++) {
        f32x2 a = pfma((f & 1) ? hi2(t[f >> 1]) : lo2(t[f >> 1]), f32x2{L2E, L2E}, f32x2{nm, nm});
        e[f] = f32x2{__builtin_amdgcn_exp2f(a[0]), __builtin_amdgcn_exp2f(a[1])};
    }
    f32x2 s = ((e[0] + e[1]) + (e[2] + e[3])) + ((e[4] + e[5]) + (e[6] + e[7])) + e[8];
    const float inv = __builtin_amdgcn_rcpf(s[0] + s[1]);
#pragma unroll
    for (int f = 0; f < 9; f++) { f32x2 v = e[f] * inv; ph[f] = pk(v[0], v[1]); }
}
DI u32x4 pk8(f32x4 a, f32x4 b) { return u32x4{pk(a[0], a[1]), pk(a[2], a[3]), pk(b[0], b[1]), pk(b[2], b[3])}; }
DI u32x2 pk4(f32x4 a) { return u32x2{pk(a[0], a[1]), pk(a[2], a[3])}; }
DI u32x4 pad4(u32x2 a) { return u32x4{a[0], a[1], 0u, 0u}; }

#define OUTAUX 0
#define LATEC 0
#define EARLYPC 0
#define NPOLL 29
#define QSW 1
#define WO7 0
#define WO8 3
#define CPH 0
#define KVSLEEP 2
#define ESLEEP 1
#define FLOOD 0
#define NPHS 9
#define NPH 4
#define CODE_LINES 272
#define YA0 12

constexpr unsigned A_STG = 48128, A_WF = 3 * A_STG, A_WL = A_WF + 10240, A_KVS = A_WL + 5120, A_RAW = 2 * A_STG, A_IMG = 82944, A_G = A_WF + 5120;
constexpr unsigned L_TAG = 163520, L_CNT = 163528, L_DUMMY = 163584;
constexpr unsigned S_STG = 36864, S_TR = 110592, S_PAR = 153600;

constexpr unsigned O_TILES = 4 * 9728;
struct OutAddr { unsigned w, w4, rd[5], gl[5]; };
DI OutAddr out_addr(unsigned sb, int w, int lane) {
    OutAddr o;
    const unsigned ob = sb + A_IMG + 64 + (w & 3) * 9728, l15 = lane & 15, q4 = lane >> 4;
    o.w = ob + l15 * 304 + q4 * 16; o.w4 = ob + l15 * 304 + (q4 < 2 ? 256 + q4 * 16 : 288);
#pragma unroll
    for (int n = 0; n < 5; n++) {
        int e = 64 * (n + (w & 4)) + lane, row = e / 18, c = e - row * 18;
        o.rd[n] = ob + row * 304 + c * 16; o.gl[n] = row * 2304 + c * 16;
        asm volatile("" : "+v"(o.rd[n]), "+v"(o.gl[n]));
    }
    asm volatile("" : "+v"(o.w), "+v"(o.w4));
    return o;
}
DI void out_store(const OutAddr& o, char* op, int w) {
    const rsrc_t ro = __builtin_amdgcn_make_buffer_rsrc(op, 0, 31 * 2304 + 288, 0x00020000);
#pragma unroll
    for (int n = 0; n < 5; n++)
        if (n < 4 || w >= 4)
            __builtin_amdgcn_raw_buffer_store_b128(__builtin_bit_cast(u32x4, __builtin_bit_cast(f32x4, lds128(o.rd[n])) + __builtin_bit_cast(f32x4, lds128(o.rd[n] + O_TILES))),
                                                   ro, o.gl[n], 0, OUTAUX);
}

__global__ __launch_bounds__(512, 2) void fused_kernel(Params P) {
    __shared__ __attribute__((aligned(1024))) char smem[163840];
    const unsigned sb = (unsigned)(size_t)(LAS char*)smem;
    STAMP(0);
    const int tid = threadIdx.x, lane = tid & 63, w = uni(tid >> 6);
    const int b = blockIdx.x, xcd = b & 7, r = b >> 3;
    const char* pc; asm volatile("s_getpc_b64 %0" : "=s"(pc));
    if (EARLYPC) {
        int ln = r + 32 * (tid >> 4); if (ln >= CODE_LINES) ln = CODE_LINES - 1;
        asm volatile("s_cmp_lt_u32 %3, 4\n\ts_cbranch_scc0 1f\n\ts_mov_b32 m0, %2\n\ts_nop 0\n\tglobal_load_lds_dword %0, %1\n1:"
                     :: "v"(ln * 128), "s"(pc), "s"(sb + L_DUMMY), "s"(w) : "memory", "scc");
    }
    const int r4 = r & 3, rq = (r >> 2) & 3;
    const int Tt = (xcd & 1) * 4 + rq;
    const int R = 2 * Tt + ((xcd >> 1) & 1), Cc = (xcd >> 2) * 4 + r4;
    const int pair = xcd * 16 + (r & 15);
    const int q4 = lane >> 4, l15 = lane & 15;
    const unsigned vrow = (unsigned)((lane >> 3) * 1152 + (((lane & 7) ^ ((lane >> 4) & 3)) << 4));
    const int rt0 = 8 * R + 2 * (w & 3);
    const int tq = (lane >> 2) & 3, tp = lane & 3;
    const unsigned trow = (unsigned)(16 * (q4 >> 1) + 2 * (4 * (q4 & 1) + tq));
    const int hw = w - 4;
    const int j = (xcd >> 1) * 4 + r4, jq = j ^ QSW;
    const int half = xcd & 1, su = (xcd >> 1) * 16 + (r - 16);
    const int g = (su * (half ? 43 : 37)) >> 8, tt = su - g * (half ? 6 : 7) + (half ? 7 : 0);
    asm volatile("" : "+s"(P.x), "+s"(P.bre), "+s"(P.bim), "+s"(P.wqkv), "+s"(P.wphi), "+s"(P.ws), "+s"(P.lre), "+s"(P.lim), "+s"(P.logdt), "+s"(P.dskip), "+s"(P.gw));
    const rsrc_t ws = __builtin_amdgcn_make_buffer_rsrc(P.ws, 0, WS_END, 0x00020000);
    if (w < 4) {
        if (r < 16) {
#pragma unroll
            for (int n = 0; n < 3; n++) dma16(P.wphi + (j >> 1) * 2592, (n < 2 || lane < 34 ? 64 * n + lane : 161) * 16, sb + A_RAW + n * 1024);
            if (w < 2) dma16(P.gw, (w == 0 || lane < 8 ? 64 * w + lane : 71) * 16, sb + A_KVS + w * 1024);
        } else {
            const char* src = lane < 24 ? (lane < 8 ? P.lre : (lane < 16 ? P.lim : P.logdt)) + (g < 9 ? g : 0) * 128 + (lane & 7) * 16
                                        : P.dskip + Cc * 144 + (lane < 33 ? lane - 24 : 8) * 16;
            asm volatile("s_mov_b32 m0, %1\n\ts_nop 0\n\tglobal_load_lds_dwordx4 %0, off" :: "v"(src), "s"(sb + S_PAR) : "memory");
        }
        int ln = r + 32 * (tid >> 4); if (ln >= CODE_LINES) ln = CODE_LINES - 1;
        if (!EARLYPC) touch64(pc, ln * 128, sb + L_DUMMY);
    }
    if (w == 2) {
        u64 ep = *(const __attribute__((address_space(4))) u64*)(size_t)(P.ws + WS_EPOCH);
        asm volatile("" : "+s"(ep));
        u32x2 t = {(unsigned)(ep + 1), (unsigned)((ep + 1) >> 32)};
        if (lane == 0) { sts128(sb + L_TAG, u32x4{t[0], t[1], 0u, 0u}); st64(ws, WS_START + b * 8, 0, t); }
    }
#define TAGS() u32x2 tag = lds64(sb + L_TAG); tag = u32x2{(unsigned)uni(tag[0]), (unsigned)uni(tag[1])}; const unsigned tg = tag[0] & 0xbfffbfffu

    if (__builtin_expect(w >= 4 && r < 16, 1)) {
        int kc = ((r4 + rq) % NPH) * 9 / NPH;
        const char* xw = P.x + (size_t)(256 * Tt + 64 * hw) * 1152;
        auto issue_x = [&](int kk, unsigned slot) {
#pragma unroll
            for (int u = 0; u < 8; u++) dma16(xw + kk * 128 + u * 8 * 1152, vrow ^ ((u & 1) << 6), sb + slot + (8 * hw + u) * 1024);
        };
        issue_x(kc, 0);
        unsigned vw[4];
#pragma unroll
        for (int u = 0; u < 4; u++) {
            int e = 64 * (hw + 4 * u) + lane;
            int pos = e / 15, c = e - pos * 15; int part = c / 5, cc = c - part * 5;
            int pp = pos & 15, m = pp >> 1;
            int k = (pos & ~15) + 8 * (m >> 2) + 4 * (pp & 1) + (m & 3);
            vw[u] = (unsigned)(k * 3456 + part * 1152 + ((72 * (part ? j : jq)) & ~15) + cc * 16);
        }
        auto issue_w = [&](int kk, unsigned slot) {
            const char* wsrc = P.wqkv + (size_t)kk * (64 * 3456);
#pragma unroll
            for (int u = 0; u < 4; u++) if (u < 3 || hw < 3) dma16(wsrc, vw[u], sb + slot + 32768 + (hw + 4 * u) * 1024);
        };
        issue_w(kc, 0);
        unsigned vh = lane < 8 ? WS_KVH + (unsigned)(jq * 8 + lane) * 128u : 0x7ffffff0u;
        STAMP(1);
        unsigned vo[6];
#pragma unroll
        for (int u = 0; u < 3; u++) {
            int e = 64 * (u == 2 ? 8 : hw + 4 * u) + lane; int pos = e / 9, c = e - pos * 9;
            int ks = pos >> 5, pp = pos & 15, m = pp >> 1;
            int kq = 2 * ((pos >> 4) & 1) + (m >> 2), je = 4 * (pp & 1) + (m & 3);
            vo[u] = (unsigned)((36 * ks + 18 * (kq >> 1) + 8 * (kq & 1) + je) * 1152 + Cc * 144 + c * 16);
            vo[3 + u] = (unsigned)((36 * (8 * ks + 2 * kq + (je >> 2)) + 18 * ((je >> 1) & 1) + 16 + (je & 1)) * 1152 + Cc * 144 + c * 16);
        }
        const int hh = hw >> 1, dt = hw & 1;
        unsigned aa = sb + A_IMG + trow * 240 + hh * 80 + tp * 8, aa2 = sb + A_IMG + trow * 240 + hh * 80 + 64 + (tp & 1) * 8;
        unsigned ab = sb + A_IMG + trow * 240 + 160 + hh * 40 + (dt ? 32 : tp * 8);
        unsigned sa[3], da[2], v0, v1;
        {
            const unsigned e0 = dt ? 320 + hh * 40 + q4 * 4 + l15 : hh * 160 + q4 * 16 + l15, st = dt ? 16 : 64;
#pragma unroll
            for (int ft = 0; ft < 3; ft++) sa[ft] = ((!dt || l15 < 4) && (ft < 2 || q4 < 2)) ? (e0 + ft * st) * 16 + WS_KV + (unsigned)(j * 8 + Tt) * KVB : 0x7ffffff0u;
            const int g0 = 104 * hw + lane;
            const bool two = lane < 40 && g0 + 64 < 400;
#pragma unroll
            for (int h2 = 0; h2 < 2; h2++) {
                int e = (h2 && two) ? g0 + 64 : g0, h, grp, d;
                if (e < 320) { h = e >= 160; grp = (e - 160 * h) >> 4; d = (e & 15) + 2 * h; }
                else { e -= 320; h = e >= 40; grp = (e - 40 * h) >> 2; e &= 3; d = e == 2 ? 18 : (e == 3 ? -1 : (h ? e : 16 + e)); }
                int sg = grp >= 5;
                da[h2] = (d < 0 || (h2 && !two)) ? sb + L_DUMMY : sb + A_KVS + ((h * 19 + d) * 2 + sg) * 48 + (grp - 5 * sg) * 8;
            }
            v0 = g0 * 16; v1 = two ? v0 + 1024 : 0x7ffffff0u;
        }
        u32x4 sel = sel_frag(l15, q4);
        asm volatile("" : "+v"(aa), "+v"(aa2), "+v"(ab), "+v"(sa[0]), "+v"(sa[1]), "+v"(sa[2]), "+v"(da[0]), "+v"(da[1]), "+v"(v0), "+v"(v1), "+v"(sel), "+v"(vh));
        asm volatile("" : "+v"(vo[0]), "+v"(vo[1]), "+v"(vo[2]), "+v"(vo[3]), "+v"(vo[4]), "+v"(vo[5]));
        unsigned slot = A_STG;
#pragma unroll 1
        for (int it = -1; it < 7; it++) {
            if (it >= 0) { wait_vm<11>(); barrier(); ITSTAMP(it); }
            kc++; if (kc >= 9) kc -= 9;
            issue_w(kc, slot); issue_x(kc, slot);
            slot += A_STG; if (slot == 3 * A_STG) slot = 0;
        }
        wait_vm<11>(); barrier();
        ITSTAMP(7);
        wait_vm<0>();
#pragma unroll 1
        for (int ph = 0, u0 = 0; ph < 2; ph++) {
            barrier();
            ITSTAMP(8 + ph);
#pragma unroll 1
            for (; u0 < (ph == 0 ? WO8 : 9); u0++) {
                int u = u0 + 2 * rq; if (u >= 9) u -= 9;
                const char* src = P.wo + (u < 8 ? u : 0) * 72 * 1152;
                const unsigned dst = sb + (u < 8 ? u + 1 : 0) * 9216;
                dma16(src, u < 8 ? vo[0] : vo[3], dst + hw * 1024);
                dma16(src, u < 8 ? vo[1] : vo[4], dst + (hw + 4) * 1024);
                if ((u & 3) == hw) dma16(src, u < 8 ? vo[2] : vo[5], dst + 8192);
            }
        }
        if (tid < 256 + 232) sts128(sb + A_KVS + (tid - 256) * 16, u32x4{0u, 0u, 0u, 0u});
        TAGS();
        STAMP(3);
        __builtin_amdgcn_s_setprio(2);
        barrier();
        STAMP(4);
        {
            u32x4 fa[8][3], fb[8];
#pragma unroll
            for (int ks = 0; ks < 8; ks++) {
                fb[ks] = ldstr2(ab + ks * 7680, 240);
                fa[ks][0] = ldstr2(aa + ks * 7680, 240); fa[ks][1] = ldstr2(aa + 32 + ks * 7680, 240); fa[ks][2] = ldstr2(aa2 + ks * 7680, 240);
            }
            f32x4 kv[3] = {{0, 0, 0, 0}, {0, 0, 0, 0}, {0, 0, 0, 0}};
#pragma unroll
            for (int ks = 0; ks < 8; ks++)
#pragma unroll
                for (int ft = 0; ft < 3; ft++) kv[ft] = mfma32(fa[ks][ft], fb[ks], kv[ft]);
            __builtin_amdgcn_sched_group_barrier(0x100, 16, 0);
#pragma unroll
            for (int ks = 0; ks < 6; ks++) { __builtin_amdgcn_sched_group_barrier(0x100, 8, 0); __builtin_amdgcn_sched_group_barrier(0x8, 3, 0); }
            __builtin_amdgcn_sched_group_barrier(0x8, 6, 0);
#pragma unroll
            for (int ft = 0; ft < 3; ft++) st128(ws, sa[ft], 0, u32x4{pk(kv[ft][0], kv[ft][1]), tg, pk(kv[ft][2], kv[ft][3]), tg});
        }
        asm volatile("" ::: "memory");
        if (lane == 0 && __hip_atomic_fetch_add((LAS unsigned*)(size_t)(sb + L_CNT), 1u, __ATOMIC_RELAXED, __HIP_MEMORY_SCOPE_WORKGROUP) == 3)
            st32(ws, WS_KVH + (unsigned)(j * 8 + Tt) * 128u, 0, tg);
        STAMP(5);
        {
            u32x4 gr[2][8];
#pragma unroll
            for (int t = 0; t < 8; t++) gr[0][t] = gr[1][t] = u32x4{0u, 0u, 0u, 0u};
            const unsigned kb = WS_KV + (unsigned)jq * (8 * KVB), tg1 = v1 == 0x7ffffff0u ? 0u : tg;
            const u32x4 wsd = {(unsigned)(size_t)P.ws, (unsigned)((size_t)P.ws >> 32) & 0xffffu, WS_END, 0x00020000u};
            unsigned todo = 0xffu;
            f32x4 s0 = {0, 0, 0, 0}, s1 = {0, 0, 0, 0};
#pragma unroll 1
            for (int spin = 0; spin < SPIN_CAP; spin++) {
                asm volatile("" ::: "memory");
                __builtin_amdgcn_s_sleep(KVSLEEP);
                unsigned m = (unsigned)__ballot(ld32(ws, vh, 0) == tg) & todo;
#pragma unroll
                for (int t = 0; t < 8; t++)
                    asm volatile("s_bitcmp1_b32 %2, %3\n\ts_cbranch_scc0 1f\n\tbuffer_load_dwordx4 %0, %4, %6, %7 offen sc1\n\tbuffer_load_dwordx4 %1, %5, %6, %7 offen sc1\n1:"
                                 : "+v"(gr[0][t]), "+v"(gr[1][t]) : "s"(m), "i"(t), "v"(v0), "v"(v1), "s"(wsd), "s"(kb + t * KVB) : "memory", "scc");
                todo &= ~m;
                if (!todo) {
                    STAMP(7);
                    asm volatile("s_waitcnt vmcnt(0)" : "+v"(gr[0][0]), "+v"(gr[0][1]), "+v"(gr[0][2]), "+v"(gr[0][3]), "+v"(gr[0][4]), "+v"(gr[0][5]), "+v"(gr[0][6]), "+v"(gr[0][7]),
                                 "+v"(gr[1][0]), "+v"(gr[1][1]), "+v"(gr[1][2]), "+v"(gr[1][3]), "+v"(gr[1][4]), "+v"(gr[1][5]), "+v"(gr[1][6]), "+v"(gr[1][7]) :: "memory");
                    unsigned x = 0;
                    s0 = s1 = f32x4{0, 0, 0, 0};
#pragma unroll
                    for (int t = 0; t < 8; t++) {
                        s0 = mfma32(sel, gr[0][t], s0); s1 = mfma32(sel, gr[1][t], s1);
                        x |= tagdiff(gr[0][t], tg) | tagdiff(gr[1][t], tg1);
                    }
                    asm volatile("" : "+v"(s0), "+v"(s1), "+v"(x));
                    if (__all(x == 0)) break;
                    todo = 0xffu;
                }
            }
            wait_vm<0>();
            sts64(da[0], pk4(s0)); sts64(da[1], pk4(s1));
        }
        STAMP(6);
        __builtin_amdgcn_s_setprio(0);
        barrier();
        float g2[2];
#pragma unroll
        for (int m = 0; m < 2; m++) g2[m] = *(LAS float*)(size_t)(sb + A_G + (128 * (R & 1) + 32 * hw + 16 * m + l15) * 4);
        OutAddr oa = out_addr(sb, w, lane);
        barrier();
        barrier();
        {
            poll_flags(ws, lane ? 0x7ffffff0u : WS_YAF + (unsigned)(pair * 4 + hw) * 128u, lane == 0, tag);
            const unsigned yso = WS_YA + (unsigned)(pair * 4 + hw) * 5120u;
#pragma unroll
            for (int t = 0; t < 5; t++) {
                u32x4 v = ld128(ws, (t < 4 || lane < 32) ? lane * 16 : 0x7ffffff0u, yso + t * 1024);
#pragma unroll
                for (int m = 0; m < 2; m++)
                    sts128((t < 4 ? oa.w + t * 64 : oa.w4) + m * 4864 + O_TILES,
                           __builtin_bit_cast(u32x4, f32x4{bflo(v[2 * m]), bfhi(v[2 * m]), bflo(v[2 * m + 1]), bfhi(v[2 * m + 1])} * g2[m]));
            }
        }
        STAMP(12);
        barrier();
        out_store(oa, (char*)P.out + (size_t)(16 * rt0) * 2304 + 288 * Cc, w);
    } else if (r < 16) {
        wait_vm<EARLYPC ? 0 : 1>();
#pragma unroll 1
        for (int n = w; n < 10; n += 4) {
            const int side = n >= 5, u = n - 5 * side;
            int oq = l15 >> 2, hh = oq >> 1, f = 4 * u + (l15 & 3);
            unsigned sg = (oq & 1) ? 0x8000u : 0u, raw = sb + A_RAW + ((side ? j : jq) & 1) * 1296;
            unsigned v[12];
#pragma unroll
            for (int e = 0; e < 12; e++) {
                int col = e < 4 ? 4 * q4 + e : (e < 8 ? 12 + 4 * q4 + e : 24 + e);
                int hc = col >= 18 ? 1 : 0;
                bool ok = hc == hh && f < 18 && (e < 8 || q4 == side);
                unsigned t = *(LAS u16*)(size_t)(raw + (ok ? (col * 18 + f) * 2 : 0));
                v[e] = ok ? (t ^ sg) : 0u;
            }
            if (n == 4 && f == 18 && q4 == 3) v[8] = 0x3f80u;
            sts128(sb + A_WF + n * 1024 + lane * 16, u32x4{v[0] | v[1] << 16, v[2] | v[3] << 16, v[4] | v[5] << 16, v[6] | v[7] << 16});
            sts64(sb + A_WL + n * 512 + lane * 8, u32x2{v[8] | v[9] << 16, v[10] | v[11] << 16});
        }
        f32x4 acc[7][4];
#pragma unroll
        for (int i = 0; i < 7; i++)
#pragma unroll
            for (int c = 0; c < 4; c++) { acc[i][c] = f32x4{0, 0, 0, 0}; asm volatile("" : "+v"(acc[i][c])); }
        const unsigned ax = sb + (unsigned)((64 * w + l15) * 128 + ((q4 ^ ((lane >> 1) & 7)) << 4));
        const unsigned aw = sb + 32768 + trow * 240 + (j & 1) * 8 + tp * 8;
        const unsigned awq = sb + 32768 + trow * 240 + (jq & 1) * 8 + tp * 8;
        const unsigned aw6 = sb + 32768 + trow * 240 + ((tp ? j : jq) & 1) * 8 + 64 + 80 * (tp > 2 ? 2 : tp);
        unsigned irow = sb + A_IMG + (unsigned)((64 * w + l15) * 240);
        unsigned ia = irow + q4 * 40, iv = irow + 160 + q4 * 8, il = irow + (q4 == 0 ? 232 : (q4 == 2 ? 224 : q4 * 40)), wfa = sb + A_WF + lane * 16, wla = sb + A_WL + lane * 8;
        asm volatile("" : "+v"(ia), "+v"(iv), "+v"(il), "+v"(wfa), "+v"(wla));
        u32x4 xf[2][4], wf[2][7], gf[2];
        int kc = ((r4 + rq) % NPH) * 9 / NPH;
        const unsigned ag = sb + A_KVS + q4 * 16;
        auto rd = [&](int s, unsigned slot) {
            gf[s] = lds128(ag + kc * 128 + s * 64);
#pragma unroll
            for (int c = 0; c < 4; c++) xf[s][c] = lds128((ax ^ (s << 6)) + slot + c * 2048);
#pragma unroll
            for (int i = 0; i < 7; i++)
                wf[s][i] = i < 6 ? ldstr2((i < 2 ? awq : aw) + slot + s * 7680 + (i >> 1) * 80 + (i & 1) * 32, 240) : ldstr2(aw6 + slot + s * 7680, 240);
        };
        auto mm = [&](int s) {
            if (l15 >= 12) wf[s][6] = gf[s];
#pragma unroll
            for (int i = 0; i < 7; i++)
#pragma unroll
                for (int c = 0; c < 4; c++) acc[i][c] = mfma32(wf[s][i], xf[s][c], acc[i][c]);
        };
        STAMP(1);
        barrier();
        STAMP(2); CSTART;
        unsigned slot = 0;
        rd(0, 0);
#pragma unroll 1
        for (int it = 0; it < 8; it++) {
            SCHED_ALL();
            wait_both<63>();
            SCHED_ALL();
            rd(1, slot); mm(0); interleave<28, 19, 1>();
            SCHED_ALL();
            barrier();
            slot += A_STG; if (slot == 3 * A_STG) slot = 0;
            kc++; if (kc >= 9) kc = 0;
            wait_both<63>();
            SCHED_ALL();
            rd(0, slot); mm(1); interleave<28, 19, 1>();
            SCHED_ALL();
        }
        rd(1, 2 * A_STG); mm(0); interleave<28, 19, 1>();
        SCHED_ALL();
        barrier();
        wait_both<63>();
        SCHED_ALL();
        u32x4 am[5], al[5];
#pragma unroll
        for (int u = 0; u < 5; u++) { am[u] = lds128(wfa + 5120 + u * 1024); al[u] = pad4(lds64(wla + 2560 + u * 512)); }
        if (l15 >= 12) wf[1][6] = gf[1];
#pragma unroll
        for (int i0 = 0; i0 < 7; i0++) {
            const int i = i0 < 2 ? i0 + 2 : (i0 == 2 ? 6 : (i0 < 5 ? i0 + 1 : i0 - 5));
#pragma unroll
            for (int c = 0; c < 4; c++) acc[i][c] = mfma32(wf[1][i], xf[1][c], acc[i][c]);
        }
        STAMP(3); CEND;
        u32x4 bl[4];
        unsigned ph[4][9];
        {
            f32x4 t[4][5];
            bl[0] = pad4(pk4(acc[6][0])); feat_mm(pk8(acc[2][0], acc[3][0]), bl[0], am, al, t[0]);
#pragma unroll
            for (int c = 0; c < 4; c++) {
                SCHED_ALL();
                if (c < 3) { bl[c + 1] = pad4(pk4(acc[6][c + 1])); feat_mm(pk8(acc[2][c + 1], acc[3][c + 1]), bl[c + 1], am, al, t[c + 1]); }
                sts64(iv + c * 3840, pk4(acc[4][c]));
                const u32x2 v5 = pk4(acc[5][c]);
                sts64(iv + c * 3840 + 32, u32x2{v5[0], q4 ? v5[1] : 0x3f80u});
                sts64(il + c * 3840, q4 ? u32x2{bl[c][0], bl[c][1]} : u32x2{v5[1], 0x3f80u});
                feat_sm(t[c], ph[c]);
#pragma unroll
                for (int m = 0; m < 4; m++) sts64(ia + c * 3840 + m * 8, u32x2{ph[c][2 * m], ph[c][2 * m + 1]});
                sts64(ia + c * 3840 + 32, u32x2{ph[c][8], 0u});
            }
        }
        barrier();
        STAMP(4);
        TAGS();
        float g1[4];
#pragma unroll
        for (int u = 0; u < 5; u++) { am[u] = lds128(wfa + u * 1024); al[u] = pad4(lds64(wla + u * 512)); }
        u32x4 p0[4], p1[4], p2[4];
        {
            f32x4 t[4][5];
            feat_mm(pk8(acc[0][0], acc[1][0]), bl[0], am, al, t[0]);
#pragma unroll
            for (int c = 0; c < 4; c++) {
                SCHED_ALL();
                if (c < 3) feat_mm(pk8(acc[0][c + 1], acc[1][c + 1]), bl[c + 1], am, al, t[c + 1]);
                feat_sm(t[c], ph[c]);
                g1[c] = __builtin_amdgcn_rcpf(1.f + __builtin_amdgcn_exp2f(t[c][4][2] * 1.44269504088896341f));
                *(LAS float*)(size_t)(sb + A_G + (64 * w + 16 * c + l15) * 4) = 1.f - g1[c];
                p0[c] = u32x4{ph[c][0], ph[c][1], ph[c][2], ph[c][3]}; p1[c] = u32x4{ph[c][4], ph[c][5], ph[c][6], ph[c][7]}; p2[c] = u32x4{ph[c][8], 0u, 0u, 0u};
                asm volatile("" : "+v"(p0[c]), "+v"(p1[c]), "+v"(p2[c]));
            }
        }
        unsigned ka[3], oa, ola;
        {
            const int oq = l15 >> 2, hh = oq >> 1, sl = l15 & 3;
            const bool on = (q4 >> 1) == hh;
#pragma unroll
            for (int u = 0; u < 3; u++) {
                int d = u < 2 ? 8 * (oq & 1) + 4 * u + sl : (sl < 3 ? 16 + sl : 18);
                ka[u] = sb + A_KVS + (on ? (unsigned)(((hh * 19 + d) * 2 + (q4 & 1)) * 48) : 3648u);
            }
            oa = lane * 16 + WS_O + (unsigned)((16 * Tt + 4 * w) * 16 + jq) * 1024u;
            ola = q4 ? 0x7ffffff0u : (unsigned)(l15 * 8);
            asm volatile("" : "+v"(ka[0]), "+v"(ka[1]), "+v"(ka[2]), "+v"(oa), "+v"(ola));
        }
        f32x4 y[2][5];
#pragma unroll
        for (int t = 0; t < 5; t++) { y[0][t] = y[1][t] = f32x4{0, 0, 0, 0}; asm volatile("" : "+v"(y[0][t]), "+v"(y[1][t])); }
        OutAddr oad = out_addr(sb, w, lane);
        const unsigned ab = sb + trow * 144 + tp * 8;
        unsigned vl0 = lane * 16, vl1 = vl0 + 16384, vll = q4 * 256 + l15 * 8;
        asm volatile("" : "+v"(vl0), "+v"(vl1), "+v"(vll));
        STAMP(6);
        barrier();
        STAMP(7);
        {
            u32x4 a0[3], a1[3], a2[3];
#pragma unroll
            for (int u = 2; u >= 0; u--) { a0[u] = lds128(ka[u]); a1[u] = lds128(ka[u] + 16); a2[u] = lds128(ka[u] + 32); }
            f32x4 nm[4][3];
#pragma unroll
            for (int c = 0; c < 4; c++)
#pragma unroll
                for (int u = 2; u >= 0; u--) nm[c][u] = mfma32(a2[u], p2[c], mfma32(a1[u], p1[c], mfma32(a0[u], p0[c], f32x4{0, 0, 0, 0})));
#pragma unroll
            for (int c = 0; c < 4; c++) {
                float inv = __builtin_amdgcn_rcpf(nm[c][2][2] + 1e-6f) * g1[c];
                st128(ws, oa, c * 16384, pk8(nm[c][0] * inv, nm[c][1] * inv));
                unsigned lo = pk(nm[c][2][0] * inv, nm[c][2][1] * inv);
                auto sw = __builtin_amdgcn_permlane32_swap(lo, lo, false, false);
                st64(ws, ola, WS_OL + (unsigned)((16 * Tt + 4 * w + c) * 16 + jq) * 128u, u32x2{sw[0], sw[1]});
            }
        }
        STAMP(5);
        asm volatile("" ::: "memory");
        u32x4 af[6][2], wv[2][5];
#pragma unroll
        for (int t = 0; t < 5; t++) wv[0][t] = ldstr2(ab + t * 32, 144);
        wait_vm<0>();
        barrier();
        STAMP(8);
        if (w == 0) {
            if (lane == 0) st64(ws, WS_OFLAG + (unsigned)(Tt * 16 + j) * 128u, 0, tag);
            poll_flags(ws, lane < 16 ? WS_OFLAG + (unsigned)(Tt * 16 + l15) * 128u : 0x7ffffff0u, lane < 16, tag);
        }
        barrier();
        STAMP(10);
        {
            unsigned os = WS_O + (unsigned)rt0 * 16384u;
#pragma unroll
            for (int k = 0; k < 5; k++) {
#pragma unroll
                for (int m = 0; m < 2; m++) {
                    if (k < 2) {
                        const unsigned so = WS_OL + (unsigned)rt0 * 2048u, o = m * 2048 + k * 1024;
                        u32x2 lo = ld64(ws, vll + o, so), hi = ld64(ws, vll + o + 128, so);
                        af[k][m] = u32x4{lo[0], lo[1], hi[0], hi[1]};
                    } else af[k][m] = ld128(ws, (m ? vl1 : vl0) + (k - 2) * 1024, os);
                }
                SCHED_ALL();
            }
            os += 3072;
            unsigned a = ab;
#pragma unroll 1
            for (int it = 0; it < 3; it++) {
                const unsigned e0 = it == 2 ? 0x7fff0000u : vl0, e1 = it == 2 ? 0x7fff0000u : vl1;
                unsigned os1 = os + 4096;
                asm volatile("" : "+v"(a), "+s"(os), "+s"(os1));
#pragma unroll
                for (int i = 0; i < 6; i++) {
                    SCHED_ALL();
                    wait_both<8>();
                    SCHED_ALL();
                    p2_step(y, wv[i & 1], af[i],
                        [&](int t) { wv[(i + 1) & 1][t] = ldstr2(a + (i + 1) * 4608 + t * 32, 144); },
                        [&](int m) { af[(i + 5) % 6][m] = ld128(ws, (i == 0 ? (m ? vl1 : vl0) : (m ? e1 : e0)) + (i & 3) * 1024, i < 4 ? os : os1); });
                }
                os += 6144; a += 27648;
            }
            mfma_drain();
        }
        STAMP(11);
#pragma unroll
        for (int m = 0; m < 2; m++)
#pragma unroll
            for (int t = 0; t < 5; t++) sts128((t < 4 ? oad.w + t * 64 : oad.w4) + m * 4864, __builtin_bit_cast(u32x4, y[m][t]));
        barrier();
        STAMP(12);
        out_store(oad, (char*)P.out + (size_t)(16 * rt0) * 2304 + 288 * Cc, w);
        STAMP(13);
    } else {
        const unsigned hfo = WS_HFLAG + (unsigned)(((lane & 16) ? (128 * R + 127) / 160 : (128 * R) / 160) * 9 + (l15 < 9 ? l15 : 8)) * 128u;
        if (g >= 9) {
            const int sp = half ? su - 53 : 0;
            if (SPDELAY) __builtin_amdgcn_s_sleep(SPDELAY);
#pragma unroll 1
            for (int n = sp * 8 + w; n < 243 + NPOLL; n += 88) {
                int m = n - NPOLL, a = m / 81;
                touch64(m < 0 ? P.ws + WS_HFLAG + n * 8192 : (a == 0 ? P.cre : (a == 1 ? P.cim : P.wo)) + (m - 81 * a) * 8192, lane * 128, sb + L_DUMMY);
            }
            STAMP(2);
        }
        if (w >= 4) {
            auto dma_xt = [&]() {
#pragma unroll
                for (int n = 0; n < 5; n++) {
                    int e = 64 * n + lane; if (e > 287) e = 287;
                    int row = e / 9, c = e - row * 9;
                    dma16(P.x + (size_t)(16 * rt0) * 1152 + Cc * 144, row * 1152 + c * 16, sb + S_TR + hw * 10496 + n * 1024);
                }
            };
            if (g < 9) {
                int kc = ((tt + g) % NPHS) * 9 / NPHS;
                const size_t bo = (size_t)(64 * g + 16 * hw) * 1152;
                auto issue_b = [&](int kk, unsigned slot) {
#pragma unroll
                    for (int e = 0; e < 4; e++)
                        dma16(((e >> 1) ? P.bim : P.bre) + bo + kk * 128 + (e & 1) * 8 * 1152, vrow ^ ((e & 1) << 6), sb + slot + 20480 + (4 * hw + e) * 1024);
                };
                issue_b(kc, 0);
                unsigned vx[5];
#pragma unroll
                for (int e = 0; e < 5; e++) {
                    int n = 5 * hw + e;
                    int t = 160 * tt + 80 * (n & 1) + 2 * (n >> 1) + 40 * (lane >> 5) + 20 * ((lane >> 3) & 1) + ((lane >> 4) & 1);
                    if (t > 2047) t = 2047;
                    vx[e] = (unsigned)(t * 1152 + (((lane & 7) ^ ((lane >> 4) & 3) ^ ((n & 1) << 2)) << 4));
                }
                auto issue_x = [&](int kk, unsigned slot) {
#pragma unroll
                    for (int e = 0; e < 5; e++) dma16(P.x + kk * 128, vx[e], sb + slot + (5 * hw + e) * 1024);
                };
                issue_x(kc, 0);
                STAMP(1);
                unsigned slot = S_STG;
#pragma unroll 1
                for (int it = -1; it < 7; it++) {
                    if (it >= 0) { wait_vm<9>(); barrier(); ITSTAMP(it); }
                    kc++; if (kc >= 9) kc -= 9;
                    issue_b(kc, slot); issue_x(kc, slot);
                    slot += S_STG; if (slot == 3 * S_STG) slot = 0;
                }
            }
            dma_xt();
            auto dma_c2 = [&](int e) { dma_cimg<9, 0>(P, 2 * e + (hw >> 1), hw & 1, Cc, vrow, sb + e * S_STG + hw * 9216); };
            if (CPH == 0) {
#pragma unroll 1
                for (int e = 0; e < 3; e++) {
                    if (e == 0) wait_vm<14>();
                    if (e == 1) wait_vm<9>();
                    if (g < 9 || e == 2) barrier();
                    dma_c2(e);
                }
            } else {
                if (g < 9) { wait_vm<14>(); barrier(); ITSTAMP(7); wait_vm<5>(); barrier(); ITSTAMP(8); }
                wait_vm<0>();
#pragma unroll 1
                for (int e = 0; e < 3; e++) {
                    if (e == 0 || (CPH == 2 && e == 1 && g < 9)) barrier();
                    dma_c2(e);
                }
            }
            TAGS();
            STAMP(3);
            if (g >= 9 && !half && hw == 1) {
#pragma unroll 1
                for (int e = 0; e < 4; e++) poll_flags(ws, WS_START + (unsigned)(e * 64 + lane) * 8u, true, tag);
                if (lane == 0) st64(ws, WS_EPOCH, 0, tag);
            }
            barrier();
            if (hw == 3) {
                if (g < 9 && lane == 0) st64(ws, WS_HFLAG + (unsigned)(tt * 9 + g) * 128u, 0, tag);
            }
            if (LATEC) dma_cimg<9, 0>(P, 6 + (hw >> 1), hw & 1, Cc, vrow, sb + 3 * S_STG + hw * 9216);
            if (hw == 3) {
                poll_flags(ws, lane < 32 ? hfo : 0x7ffffff0u, lane < 32, tag);
            }
            wait_vm<0>();
            STAMP(7);
            barrier();
            if (!LATEC) __builtin_amdgcn_s_sleep(8);
            if (!LATEC) dma_cimg<9, 2>(P, 6 + (hw >> 1), hw & 1, Cc, vrow, sb + 3 * S_STG + hw * 9216);
            barrier();
            if (hw < 2) dma_cimg<9, 1>(P, 8, hw, Cc, vrow, sb + hw * 9216);
            wait_vm<0>();
            barrier();
        } else {
            f32x4 y[2][5];
            unsigned dv[5];
            wait_vm<EARLYPC ? 0 : 1>();
#pragma unroll
            for (int t = 0; t < 5; t++) dv[t] = *(LAS u16*)(size_t)(sb + S_PAR + 384 + (16 * t + l15) * 2);
            auto prep_d = [&]() {
#pragma unroll
                for (int t = 0; t < 5; t++) {
                    bool on = q4 == 2 * (t & 1) + (l15 >> 3) && (t < 4 || l15 < 8);
                    dv[t] = on ? dv[t] << (16 * (l15 & 1)) : 0u;
                    asm volatile("" : "+v"(dv[t]));
                }
            };
            auto init_y = [&]() {
                u32x4 xb[2][3];
#pragma unroll
                for (int m = 0; m < 2; m++)
#pragma unroll
                    for (int s = 0; s < 3; s++) xb[m][s] = lds128(sb + S_TR + w * 10496 + (16 * m + l15) * 144 + s * 64 + q4 * 16);
                const int dw = (l15 & 7) >> 1;
#pragma unroll
                for (int t = 0; t < 5; t++) {
                    u32x4 a = {dw == 0 ? dv[t] : 0u, dw == 1 ? dv[t] : 0u, dw == 2 ? dv[t] : 0u, dw == 3 ? dv[t] : 0u};
#pragma unroll
                    for (int m = 0; m < 2; m++) { y[m][t] = mfma32(a, xb[m][t >> 1], f32x4{0, 0, 0, 0}); asm volatile("" : "+v"(y[m][t])); }
                }
            };
            if (g < 9) {
                f32x2 lam, coef, l20, l40, pw, wq, l640;
                {
                    const unsigned pa = sb + S_PAR + (16 * w + l15) * 2;
                    const unsigned ra = *(LAS u16*)(size_t)pa, rb = *(LAS u16*)(size_t)(pa + 128), rd = *(LAS u16*)(size_t)(pa + 256);
                    float a = bf2f(ra), bb = bf2f(rb);
                    float dt = bflo(pk(expf(bf2f(rd)), 0.f));
                    float e = expf(dt * a), sn, cs;
                    sincosf(dt * bb, &sn, &cs);
                    lam = f32x2{e * cs, e * sn};
                    float nr = lam[0] - 1.f, ni = lam[1], inv = 2.f / (a * a + bb * bb);
                    coef = f32x2{(nr * a + ni * bb) * inv, (ni * a - nr * bb) * inv};
                    f32x2 l2 = cmul(lam, lam), l4 = cmul(l2, l2), l5 = cmul(l4, lam), l10 = cmul(l5, l5);
                    l20 = cmul(l10, l10); l40 = cmul(l20, l20);
                    f32x2 l80 = cmul(l40, l40), l160 = cmul(l80, l80), l320 = cmul(l160, l160);
                    l640 = cmul(l320, l320);
                    const f32x2 one = {1.f, 0.f};
                    pw = cmul((q4 & 1) ? l40 : one, (q4 & 2) ? l80 : one);
                    wq = cmul((q4 & 1) ? l160 : one, (q4 & 2) ? l320 : one);
                    asm volatile("" : "+v"(lam), "+v"(coef), "+v"(l20), "+v"(l40), "+v"(pw), "+v"(wq), "+v"(l640));
                }
                prep_d();
                f32x4 acc[10][2];
#pragma unroll
                for (int i = 0; i < 10; i++) { acc[i][0] = acc[i][1] = f32x4{0, 0, 0, 0}; asm volatile("" : "+v"(acc[i][0]), "+v"(acc[i][1])); }
                const unsigned sw = (unsigned)(l15 * 128 + ((q4 ^ ((lane >> 1) & 7)) << 4));
                const unsigned ax = sb + sw, ab = sb + 20480 + w * 4096 + sw;
                u32x4 xf[2][10], bf[2][2];
                auto rd = [&](int s, unsigned slot) {
#pragma unroll
                    for (int c = 0; c < 2; c++) bf[s][c] = lds128((ab ^ (s << 6)) + slot + c * 2048);
#pragma unroll
                    for (int i = 0; i < 10; i++) xf[s][i] = lds128((ax ^ (s << 6)) + slot + i * 2048);
                };
                auto mm = [&](int s) {
#pragma unroll
                    for (int i = 0; i < 10; i++) { acc[i][0] = mfma32(xf[s][i], bf[s][0], acc[i][0]); acc[i][1] = mfma32(xf[s][i], bf[s][1], acc[i][1]); }
                };
                STAMP(1);
                barrier();
                STAMP(2); CSTART;
                unsigned slot = 0;
                const unsigned tg = (unsigned)uni(*(LAS unsigned*)(size_t)(sb + L_TAG)) & 0xbfffbfffu;
                rd(0, 0);
#pragma unroll 1
                for (int it = 0; it < 8; it++) {
                    SCHED_ALL();
                    wait_both<63>();
                    SCHED_ALL();
                    rd(1, slot); mm(0); interleave<20, 12, 1>();
                    SCHED_ALL();
                    barrier();
                    slot += S_STG; if (slot == 3 * S_STG) slot = 0;
                    wait_both<63>();
                    SCHED_ALL();
                    rd(0, slot); mm(1); interleave<20, 12, 1>();
                    SCHED_ALL();
                }
                rd(1, 2 * S_STG); mm(0); interleave<20, 12, 1>();
                SCHED_ALL();
                barrier();
                wait_both<63>();
                SCHED_ALL();
                mm(1);
                STAMP(3); CEND;
                const f32x2 lr = {lam[0], lam[0]}, li = {lam[1], lam[1]};
                f32x2 er = lo2(acc[0][0]), ei = lo2(acc[0][1]);
#pragma unroll
                for (int n = 1; n < 20; n++) {
                    f32x2 ur = (n & 1) ? hi2(acc[n >> 1][0]) : lo2(acc[n >> 1][0]), ui = (n & 1) ? hi2(acc[n >> 1][1]) : lo2(acc[n >> 1][1]);
                    f32x2 nr = pfma(lr, er, pfma(-li, ei, ur)), ni = pfma(lr, ei, pfma(li, er, ui));
                    er = nr; ei = ni;
                }
                const f32x2 e0 = {er[0], ei[0]};
                const f32x2 el = cfma(l20, e0, f32x2{er[1], ei[1]});
                float gr[4], gi[4];
                gather4(el[0], gr); gather4(el[1], gi);
                f32x2 pre = {0.f, 0.f}, run = {gr[0], gi[0]};
#pragma unroll
                for (int s = 1; s < 4; s++) { if (s == q4) pre = run; run = cfma(l40, run, f32x2{gr[s], gi[s]}); }
                st128(ws, q4 == 0 ? (w * 16 + l15) * 16 : 0x7ffffff0u, WS_E + (unsigned)(tt * 9 + g) * 1024u,
                      u32x4{__float_as_uint(run[0]), tg, __float_as_uint(run[1]), tg});
                asm volatile("" ::: "memory");
                STAMP(4);
                init_y();
#pragma unroll
                for (int i = 0; i < 10; i++) {
                    f32x4 ur = acc[i][0], ui = acc[i][1];
                    acc[i][0] = coef[0] * ur - coef[1] * ui;
                    acc[i][1] = -coef[0] * ui - coef[1] * ur;
                    asm volatile("" : "+v"(acc[i][0]), "+v"(acc[i][1]));
                }
                unsigned ea[3], et[3];
#pragma unroll
                for (int m = 0; m < 3; m++) { int t2 = tt - 1 - q4 - 4 * m; ea[m] = t2 >= 0 ? (unsigned)(t2 * 9216 + (w * 16 + l15) * 16) : 0x7ffffff0u; et[m] = t2 >= 0 ? tg : 0u; }
                u32x4 ev[3];
#pragma unroll 1
                for (int spin = 0; spin < SPIN_CAP; spin++) {
                    asm volatile("" ::: "memory");
#pragma unroll
                    for (int m = 0; m < 3; m++) ev[m] = ld128(ws, ea[m], WS_E + (unsigned)g * 1024u);
                    unsigned x = tagdiff(ev[0], et[0]) | tagdiff(ev[1], et[1]) | tagdiff(ev[2], et[2]);
                    asm volatile("" : "+v"(x));
                    if (__all(x == 0)) break;
                    __builtin_amdgcn_s_sleep(ESLEEP);
                }
                STAMP(5);
                if (CPH == 2) asm volatile("s_barrier" ::: "memory");
                auto evc = [&](int m) { return f32x2{__uint_as_float(ev[m][0]), __uint_as_float(ev[m][2])}; };
                f32x2 cr = cmul(wq, cfma(l640, cfma(l640, evc(2), evc(1)), evc(0)));
                cr = f32x2{sum4(cr[0]), sum4(cr[1])};
                const f32x2 s0 = cfma(pw, cr, pre), s1 = cfma(l20, s0, e0);
                const f32x2 h0 = cmul(coef, s0), h1 = cmul(coef, s1);
                f32x2 hr = {h0[0], h1[0]}, hi = {-h0[1], -h1[1]};
                const unsigned tb = sb + S_TR + w * 10496;
#pragma unroll
                for (int i = 0; i < 5; i++) {
                    f32x2 vr[4], vi[4];
#pragma unroll
                    for (int k = 0; k < 4; k++) {
                        int n = 4 * i + k;
                        f32x2 ur = (n & 1) ? hi2(acc[n >> 1][0]) : lo2(acc[n >> 1][0]), ui = (n & 1) ? hi2(acc[n >> 1][1]) : lo2(acc[n >> 1][1]);
                        f32x2 nr = pfma(lr, hr, pfma(li, hi, ur)), ni = pfma(lr, hi, pfma(-li, hr, ui));
                        hr = nr; hi = ni; vr[k] = nr; vi[k] = ni;
                    }
#pragma unroll
                    for (int s = 0; s < 2; s++) {
                        sts64(tb + l15 * 328 + q4 * 80 + s * 40 + i * 8, u32x2{pk(vr[0][s], vr[1][s]), pk(vr[2][s], vr[3][s])});
                        sts64(tb + (16 + l15) * 328 + q4 * 80 + s * 40 + i * 8, u32x2{pk(vi[0][s], vi[1][s]), pk(vi[2][s], vi[3][s])});
                    }
                }
                wait_lgkm();
                const unsigned ta = tb + (8 * q4 + tq) * 328 + tp * 8;
                const unsigned ha = lane * 16 + WS_H + (unsigned)(10 * tt * 36 + 4 * g + w) * 1024u;
#pragma unroll
                for (int i = 0; i < 10; i++) {
                    u32x4 f = ldstr2(ta + i * 32, 4 * 328);
                    st128(ws, (i < 8 || tt < 12) ? ha + i * 36864 : 0x7ffffff0u, 0, f);
                }
                STAMP(10);
                wait_vm<0>();
            } else {
                prep_d();
                barrier();
                init_y();
                wait_vm<0>();
            }
            barrier();
            STAMP(6);
            unsigned ca[4], vl0 = lane * 16, vl1 = vl0 + 36864, ya = vl0 + WS_YA + (unsigned)(pair * 4 + w) * 5120u;
#pragma unroll
            for (int s = 0; s < 4; s++) {
                ca[s] = sb + (unsigned)((q4 >> 1) * 9216 + l15 * 128 + ((((q4 & 1) + 2 * s) ^ ((l15 >> 1) & 7)) << 4));
                asm volatile("" : "+v"(ca[s]));
            }
            asm volatile("" : "+v"(vl0), "+v"(vl1), "+v"(ya));
            STAMP(11);
            barrier();
            STAMP(7);
            {
                u32x4 af[6][2], wf[2][5];
                unsigned hs = WS_H + (unsigned)rt0 * 36864u;
#pragma unroll
                for (int k = 0; k < 5; k++) {
                    af[k][0] = ld128(ws, vl0 + (k & 3) * 1024, hs + (k >> 2) * 4096); af[k][1] = ld128(ws, vl1 + (k & 3) * 1024, hs + (k >> 2) * 4096);
                    SCHED_ALL();
                }
#pragma unroll
                for (int t = 0; t < 5; t++) wf[0][t] = lds128(ca[0] + t * 2048);
                hs += 5120;
#pragma unroll 1
                for (int it = 0; it < 3; it++) {
                    const unsigned e0 = it == 2 ? 0x7fff0000u : vl0, e1 = it == 2 ? 0x7fff0000u : vl1;
                    unsigned hq[3] = {hs, hs + 4096, hs + 8192}, c2[4];
#pragma unroll
                    for (int s = 0; s < 4; s++) { c2[s] = ca[s] + (it == 2 ? -110592 : 36864); asm volatile("" : "+v"(ca[s]), "+v"(c2[s])); }
                    asm volatile("" : "+s"(hq[0]), "+s"(hq[1]), "+s"(hq[2]));
#pragma unroll
                    for (int i = 0; i < 12; i++) {
                        SCHED_ALL();
                        wait_both<8>();
                        if (i == 11) asm volatile("s_cmp_eq_u32 %0, 2\n\ts_cbranch_scc1 1f\n\ts_barrier\n1:" :: "s"(it) : "memory", "scc");
                        SCHED_ALL();
                        p2_step(y, wf[i & 1], af[i % 6],
                            [&](int t) { const int n = i + 1; wf[n & 1][t] = lds128((n < 8 ? ca[n & 3] + (n >> 2) * 18432 : c2[n & 3] + ((n - 8) >> 2) * 18432) + t * 2048); },
                            [&](int m) { af[(i + 5) % 6][m] = ld128(ws, (i < 7 ? (m ? vl1 : vl0) : (m ? e1 : e0)) + (i & 3) * 1024, hq[i >> 2]); });
                    }
                    hs += 12288;
#pragma unroll
                    for (int s = 0; s < 4; s++) ca[s] += 55296;
                }
                mfma_drain();
            }
            STAMP(8);
#pragma unroll
            for (int t = 0; t < 5; t++) st128(ws, (t < 4 || lane < 32) ? ya + t * 1024 : 0x7ffffff0u, 0, pk8(y[0][t], y[1][t]));
            const u32x2 tag = lds64(sb + L_TAG);
            wait_vm<0>();
            st64(ws, lane ? 0x7ffffff0u : 0u, WS_YAF + (unsigned)(pair * 4 + w) * 128u, tag);
            STAMP(9);
        }
    }
}

extern "C" void kernel_launch(void* const* d_in, const int* in_sizes, int n_in,
                              void* d_out, int out_size, void* d_ws, size_t ws_size,
                              hipStream_t stream) {
    Params P;
    P.x = (const char*)d_in[0];
    P.lre = (const char*)d_in[1];
    P.lim = (const char*)d_in[2];
    P.bre = (const char*)d_in[3];
    P.bim = (const char*)d_in[4];
    P.cre = (const char*)d_in[5];
    P.cim = (const char*)d_in[6];
    P.logdt = (const char*)d_in[7];
    P.dskip = (const char*)d_in[8];
    P.wqkv = (const char*)d_in[10];
    P.wphi = (const char*)d_in[11];
    P.wo = (const char*)d_in[12];
    P.gw = (const char*)d_in[13];
    P.out = (float*)d_out;
    P.ws = (char*)d_ws;
    fused_kernel<<<256, 512, 0, stream>>>(P);
}
```

```cpp
#include <hip/hip_runtime.h>
#include <stdint.h>

typedef unsigned short u16;
typedef unsigned long long u64;
typedef __bf16 bf16x2 __attribute__((ext_vector_type(2)));
typedef __bf16 bf16x4 __attribute__((ext_vector_type(4)));
typedef __bf16 bf16x8 __attribute__((ext_vector_type(8)));
typedef float f32x2 __attribute__((ext_vector_type(2)));
typedef float f32x4 __attribute__((ext_vector_type(4)));
typedef unsigned u32x2 __attribute__((ext_vector_type(2)));
typedef unsigned u32x4 __attribute__((ext_vector_type(4)));
#define LAS __attribute__((address_space(3)))
#define DI __device__ __forceinline__
typedef __amdgpu_buffer_rsrc_t rsrc_t;

constexpr unsigned WS_EPOCH = 0;
constexpr unsigned WS_START = 4096;
constexpr unsigned WS_HFLAG = 24576;
constexpr unsigned WS_OFLAG = WS_HFLAG + 16384;
constexpr unsigned WS_KVH   = WS_OFLAG + 16384;
constexpr unsigned WS_YAF   = WS_KVH + 16384;
constexpr unsigned WS_E     = WS_YAF + 65536;
static_assert(WS_E + 117 * 1024 <= 262144 && WS_HFLAG + 29 * 8192 <= 262144, "workspace map");
constexpr unsigned WS_KV    = 262144;
constexpr unsigned WS_OL    = 1245184;
constexpr unsigned WS_O     = 1507328;
constexpr unsigned WS_H     = WS_O + 128 * 16 * 1024;
constexpr unsigned WS_YA    = WS_H + 128 * 36 * 1024;
constexpr unsigned WS_END   = WS_YA + 128 * 20 * 1024;

constexpr int SPIN_CAP = 1 << 18;
constexpr int AUX_SC1 = 16, AUX_NT = 2;
constexpr unsigned OOR = 0x7ffffff0u;

struct Params {
    const char *x, *lre, *lim, *bre, *bim, *cre, *cim, *logdt, *dskip, *wqkv, *wphi, *wo, *gw;
    float* out;
    char* ws;
};

DI float bflo(unsigned v) { return __uint_as_float(v << 16); }
DI float bfhi(unsigned v) { return __uint_as_float(v & 0xffff0000u); }
DI unsigned pk(float a, float b) { f32x2 t = {a, b}; return __builtin_bit_cast(unsigned, __builtin_convertvector(t, bf16x2)); }
DI f32x4 mfma32(u32x4 a, u32x4 b, f32x4 c) {
    return __builtin_amdgcn_mfma_f32_16x16x32_bf16(__builtin_bit_cast(bf16x8, a), __builtin_bit_cast(bf16x8, b), c, 0, 0, 0);
}
DI u32x4 lds128(unsigned a) { return *(LAS u32x4*)(size_t)a; }
DI u32x2 lds64(unsigned a) { return *(LAS u32x2*)(size_t)a; }
DI void sts64(unsigned a, u32x2 v) { *(LAS u32x2*)(size_t)a = v; }
DI void sts128(unsigned a, u32x4 v) { *(LAS u32x4*)(size_t)a = v; }
DI u32x2 ldstr(unsigned a) {
    return __builtin_bit_cast(u32x2, __builtin_amdgcn_ds_read_tr16_b64_v4bf16((LAS bf16x4*)(size_t)a));
}
DI u32x4 ldstr2(unsigned a, unsigned d) { u32x2 lo = ldstr(a), hi = ldstr(a + d); return u32x4{lo[0], lo[1], hi[0], hi[1]}; }
DI void dma16(const char* sbase, unsigned voff, unsigned lds) {
    asm volatile("s_mov_b32 m0, %2\n\ts_nop 0\n\tglobal_load_lds_dwordx4 %0, %1" :: "v"(voff), "s"(sbase), "s"(lds) : "memory");
}
#define GL "global_load_lds_dwordx4 "
DI void dma3(const char* sbase, const unsigned* v, unsigned lds) {
    asm volatile("s_mov_b32 m0, %4\n\ts_nop 0\n\t" GL "%0, %3\n\t" GL "%1, %3 offset:1024\n\t" GL "%2, %3 offset:2048"
                 :: "v"(v[0]), "v"(v[1]), "v"(v[2]), "s"(sbase), "s"(lds) : "memory");
}
DI void dma4(const char* sbase, const unsigned* v, unsigned lds) {
    asm volatile("s_mov_b32 m0, %5\n\ts_nop 0\n\t" GL "%0, %4\n\t" GL "%1, %4 offset:1024\n\t" GL "%2, %4 offset:2048\n\t" GL "%3, %4 offset:3072"
                 :: "v"(v[0]), "v"(v[1]), "v"(v[2]), "v"(v[3]), "s"(sbase), "s"(lds) : "memory");
}
DI void dma2(const char* sbase, const unsigned* v, unsigned lds) {
    asm volatile("s_mov_b32 m0, %3\n\ts_nop 0\n\t" GL "%0, %2\n\t" GL "%1, %2 offset:1024" :: "v"(v[0]), "v"(v[1]), "s"(sbase), "s"(lds) : "memory");
}
template <int N> DI void wait_vm() { asm volatile("s_waitcnt vmcnt(%0)" :: "i"(N) : "memory"); }
DI void wait_lgkm() { asm volatile("s_waitcnt lgkmcnt(0)" ::: "memory"); }
DI void barrier() {
    __builtin_amdgcn_sched_barrier(0);
    asm volatile("s_waitcnt lgkmcnt(0)\n\ts_barrier" ::: "memory");
    __builtin_amdgcn_sched_barrier(0);
}
DI int uni(int v) { return __builtin_amdgcn_readfirstlane(v); }
DI u32x4 ld128(rsrc_t r, unsigned v, unsigned s) { return __builtin_amdgcn_raw_buffer_load_b128(r, v, s, AUX_SC1); }
DI unsigned ld32(rsrc_t r, unsigned v, unsigned s) { return __builtin_amdgcn_raw_buffer_load_b32(r, v, s, AUX_SC1); }
DI u32x2 ld64(rsrc_t r, unsigned v, unsigned s) { return __builtin_amdgcn_raw_buffer_load_b64(r, v, s, AUX_SC1); }
DI void st128(rsrc_t r, unsigned v, unsigned s, u32x4 d) { __builtin_amdgcn_raw_buffer_store_b128(d, r, v + s, 0, AUX_SC1); }
DI void st64(rsrc_t r, unsigned v, unsigned s, u32x2 d) { __builtin_amdgcn_raw_buffer_store_b64(d, r, v, s, AUX_SC1); }
DI void st32(rsrc_t r, unsigned v, unsigned s, unsigned d) { __builtin_amdgcn_raw_buffer_store_b32(d, r, v, s, AUX_SC1); }
DI f32x2 cmul(f32x2 a, f32x2 b) { return f32x2{a[0] * b[0] - a[1] * b[1], a[0] * b[1] + a[1] * b[0]}; }
DI f32x2 cfma(f32x2 a, f32x2 b, f32x2 c) {
    return f32x2{fmaf(a[0], b[0], fmaf(-a[1], b[1], c[0])), fmaf(a[0], b[1], fmaf(a[1], b[0], c[1]))};
}

template <int SL = 1> DI void poll_flags(rsrc_t ws, unsigned fo, bool need, u32x2 tag) {
#pragma unroll 1
    for (int spin = 0; spin < SPIN_CAP; spin++) {
        asm volatile("" ::: "memory");
        u32x2 f = ld64(ws, fo, 0);
        bool ok = !need || (f[0] == tag[0] && f[1] == tag[1]);
        if (__all(ok)) break;
        __builtin_amdgcn_s_sleep(SL);
    }
    asm volatile("" ::: "memory");
}

DI void dma_cimg(const Params& P, int grp, int comp, int Cc, const unsigned* vr4, unsigned lds) {
    const char* base = (comp ? P.cim : P.cre) + (size_t)(72 * Cc * 1152 + grp * 128);
    dma4(base, vr4, lds);
    dma4(base + 32 * 1152, vr4, lds + 4096);
    dma16(base + 64 * 1152, vr4[0], lds + 8192);
}
DI void touch64(const char* src, unsigned voff, unsigned lds) {
    asm volatile("s_mov_b32 m0, %2\n\ts_nop 0\n\tglobal_load_lds_dword %0, %1 sc1" :: "v"(voff), "s"(src), "s"(lds) : "memory");
}

#define SCHED_ALL() __builtin_amdgcn_sched_barrier(0)
#define PIN(x) do { asm volatile("" :: "v"(x)); SCHED_ALL(); } while (0)
template <int V> DI void wait_both() { __builtin_amdgcn_s_waitcnt((V & 15) | ((V >> 4) << 14) | (7 << 4)); }

DI f32x2 pfma(f32x2 a, f32x2 b, f32x2 c) { return __builtin_elementwise_fma(a, b, c); }
DI f32x2 lo2(f32x4 v) { return f32x2{v[0], v[1]}; }
DI f32x2 hi2(f32x4 v) { return f32x2{v[2], v[3]}; }
DI void swap32(float x, float& lo, float& hi) { auto r = __builtin_amdgcn_permlane32_swap(__float_as_uint(x), __float_as_uint(x), false, false); lo = __uint_as_float(r[0]); hi = __uint_as_float(r[1]); }
DI void swap16(float x, float& lo, float& hi) { auto r = __builtin_amdgcn_permlane16_swap(__float_as_uint(x), __float_as_uint(x), false, false); lo = __uint_as_float(r[0]); hi = __uint_as_float(r[1]); }
DI void gather4(float x, float (&o)[4]) { float a, b; swap32(x, a, b); swap16(a, o[0], o[1]); swap16(b, o[2], o[3]); }
DI float sum4(float x) { float a, b; swap32(x, a, b); a += b; swap16(a, a, b); return a + b; }
DI unsigned tagdiff(u32x4 v, unsigned tag) { return __builtin_amdgcn_bitop3_b32(v[1], v[3], tag, 0x7e); }
DI u32x4 sel_frag(int l15, int q4) {
    unsigned v = (l15 >> 2) == q4 ? ((l15 & 1) ? 0x3f800000u : 0x3f80u) : 0u;
    return u32x4{(l15 & 2) ? 0u : v, 0u, (l15 & 2) ? v : 0u, 0u};
}

constexpr unsigned KVB = 6400;

template <int V> struct IntC { static constexpr int value = V; };
template <int N, int I = 0, class F> DI void static_for(F f) { if constexpr (I < N) { f(IntC<I>()); static_for<N, I + 1>(f); } }
DI void mfma_ip(f32x4& c, u32x4 a, u32x4 b) { asm volatile("v_mfma_f32_16x16x32_bf16 %0, %1, %2, %0" : "+v"(c) : "v"(a), "v"(b)); }
template <int N, class MM, class FL> DI void ordered(MM mm, FL fill) {
#pragma unroll
    for (int n = 0; n < N; n++) { mm(n); SCHED_ALL(); fill(n); SCHED_ALL(); }
}
template <bool FIRST, class RD, class LD>
DI void p2_step(f32x4 (&y)[2][5], const u32x4 (&wc)[5], const u32x4 (&ac)[2], RD rd, LD ld) {
#pragma unroll
    for (int n = 0; n < 10; n++) {
        if (FIRST) asm volatile("v_mfma_f32_16x16x32_bf16 %0, %1, %2, 0" : "=&v"(y[n & 1][n >> 1]) : "v"(wc[n >> 1]), "v"(ac[n & 1]));
        else mfma_ip(y[n & 1][n >> 1], wc[n >> 1], ac[n & 1]);
        SCHED_ALL();
        if (n < 5) rd(n); else if (n < 8) ld(n - 5);
        SCHED_ALL();
    }
    asm volatile("" :: "v"(wc[0]), "v"(wc[1]), "v"(wc[2]), "v"(wc[3]), "v"(wc[4]), "v"(ac[0]), "v"(ac[1]));
}
constexpr int issued(int i, int N) { int n = 10 + 2 * i + (i < 4 ? i : 4); return n < 2 * N ? n : 2 * N; }
DI void mfma_drain() { SCHED_ALL(); asm volatile("s_nop 7\n\ts_nop 1"); SCHED_ALL(); }

DI float max3(float a, float b, float c) { return __builtin_fmaxf(__builtin_fmaxf(a, b), c); }
template <class F0, class F1, class F2>
DI void feat_sm(const f32x4 (&t)[5], unsigned (&ph)[9], F0 f0, F1 f1, F2 f2) {
#define S_ SCHED_ALL()
    float m0 = max3(t[0][0], t[0][1], t[0][2]); PIN(m0); f0(0); S_;
    float m1 = max3(t[1][0], t[1][1], t[1][2]); PIN(m1); f0(1); S_;
    m0 = max3(m0, t[0][3], t[1][3]); PIN(m0); f0(2); S_;
    m1 = max3(m1, t[2][0], t[2][1]); PIN(m1); f0(3); S_;
    m0 = max3(m0, t[2][2], t[2][3]); PIN(m0); f0(4); S_;
    m1 = max3(m1, t[3][0], t[3][1]); PIN(m1); f0(5); S_;
    m0 = max3(m0, t[3][2], t[3][3]); PIN(m0); f0(6); S_;
    m1 = max3(m1, t[4][0], t[4][1]); asm volatile("" :: "v"(t[4])); PIN(m1); f0(7); S_;
    m0 = max3(m0, m1, t[0][0]); PIN(m0); f0(8); S_;
    const float L2E = 1.44269504088896341f;
    const float nm = -m0 * L2E;
    f32x2 e[9];
#pragma unroll
    for (int f = 0; f < 9; f++) e[f] = pfma((f & 1) ? hi2(t[f >> 1]) : lo2(t[f >> 1]), f32x2{L2E, L2E}, f32x2{nm, nm});
#pragma unroll
    for (int f = 0; f < 9; f++) asm volatile("" :: "v"(e[f]));
    S_;
#pragma unroll
    for (int f = 0; f < 9; f++) {
        f1(f); S_;
        e[f][0] = __builtin_amdgcn_exp2f(e[f][0]); PIN(e[f][0]);
        if (f == 7) { f1(9); S_; }
        e[f][1] = __builtin_amdgcn_exp2f(e[f][1]); PIN(e[f][1]);
    }
    f32x2 a0 = e[0] + e[1], a1 = e[2] + e[3], a2 = e[4] + e[5], a3 = e[6] + e[7];
    asm volatile("" :: "v"(a0), "v"(a1), "v"(a2), "v"(a3)); S_;
    a0 += a1; a2 += a3; asm volatile("" :: "v"(a0), "v"(a2)); S_; f2(0); S_;
    a0 += e[8]; PIN(a0); f2(1); S_;
    a0 += a2; PIN(a0); f2(2); S_;
    const float inv = __builtin_amdgcn_rcpf(a0[0] + a0[1]); PIN(inv); f2(3); S_;
#pragma unroll
    for (int f = 0; f < 9; f++) e[f] *= inv;
#pragma unroll
    for (int f = 0; f < 9; f++) asm volatile("" :: "v"(e[f]));
    S_;
#pragma unroll
    for (int f = 0; f < 9; f++) ph[f] = pk(e[f][0], e[f][1]);
#undef S_
}
DI void feat_mm1(int n, u32x4 bm, u32x4 bl, const u32x4 (&am)[5], const u32x4 (&al)[5], f32x4 (&t)[5]) {
    if (n < 5) t[n] = mfma32(am[n], bm, f32x4{0, 0, 0, 0}); else t[n - 5] = mfma32(al[n - 5], bl, t[n - 5]);
}
DI u32x4 pk8(f32x4 a, f32x4 b) { return u32x4{pk(a[0], a[1]), pk(a[2], a[3]), pk(b[0], b[1]), pk(b[2], b[3])}; }
DI u32x2 pk4(f32x4 a) { return u32x2{pk(a[0], a[1]), pk(a[2], a[3])}; }
DI u32x4 pad4(u32x2 a) { return u32x4{a[0], a[1], 0u, 0u}; }

constexpr unsigned A_STG = 48128, A_IMG = 82944, A_WP = 3 * A_STG, A_GW = A_WP + 6144, A_G = A_GW + 2048, A_KVS = A_G + 1024;
constexpr unsigned L_TAG = 163520, L_DONE = 163536, L_DUMMY = 163584;
constexpr unsigned S_STG = 36864, S_TR = 110592, S_PAR = 153600;

constexpr unsigned A_OUT = A_IMG + 64;

__global__ __launch_bounds__(512, 2) void fused_kernel(Params P) {
    __shared__ __attribute__((aligned(1024))) char smem[163840];
    const unsigned sb = (unsigned)(size_t)(LAS char*)smem;
    const int tid = threadIdx.x, lane = tid & 63, w = uni(tid >> 6);
    const int b = blockIdx.x, xcd = b & 7, r = b >> 3;
    const char* pc; int code_len;
    asm volatile("s_getpc_b64 %0\n.Lfk_begin:\n\ts_mov_b32 %1, .Lfk_end-.Lfk_begin\n\ts_max_i32 %1, %1, .Lfk_end2-.Lfk_begin\n\ts_max_i32 %1, %1, .Lfk_end3-.Lfk_begin" : "=s"(pc), "=s"(code_len) :: "scc");
    {
        int lo = (r + 32 * (tid >> 4)) * 128; if (lo > code_len) lo = code_len;
        asm volatile("s_cmp_lt_u32 %3, 4\n\ts_cbranch_scc0 1f\n\ts_mov_b32 m0, %2\n\ts_nop 0\n\tglobal_load_lds_dword %0, %1\n1:"
                     :: "v"(lo), "s"(pc), "s"(sb + L_DUMMY), "s"(w) : "memory", "scc");
    }
    const int r4 = r & 3, rq = (r >> 2) & 3;
    const int Tt = (xcd & 1) * 4 + rq;
    const int R = 2 * Tt + ((xcd >> 1) & 1), Cc = (xcd >> 2) * 4 + r4;
    const int pair = xcd * 16 + (r & 15);
    const int rota = (r4 & 1) * 2;
    const int q4 = lane >> 4, l15 = lane & 15;
    const unsigned vrow = (unsigned)((lane >> 3) * 1152 + (((lane & 7) ^ ((lane >> 4) & 3)) << 4));
    const unsigned vr4[4] = {vrow, (vrow ^ 64) + 8192, vrow + 16384, (vrow ^ 64) + 24576};
    const int rt0 = 8 * R + 2 * (w & 3);
    const int tq = (lane >> 2) & 3, tp = lane & 3;
    const unsigned trow = (unsigned)(16 * (q4 >> 1) + 2 * (4 * (q4 & 1) + tq));
    const int hw = w - 4;
    const int j = (xcd >> 1) * 4 + r4, jq = j ^ 1;
    const int half = xcd & 1, su = (xcd >> 1) * 15 + (r - 16), s5 = su - (half ? 30 : 35);
    const bool seven = (s5 < 0) != half;
    const int sv = s5 < 0 ? su : s5, gq = (sv * (seven ? 37 : 43)) >> 8;
    const int g = r == 31 ? 9 : gq + (s5 < 0 ? 0 : 5), tt = sv - gq * (seven ? 7 : 6) + (half ? (seven ? 6 : 7) : 0);
    const int sp = r == 31 ? xcd : su - (half ? 49 : 51);
#define ARGS() SCHED_ALL(); asm volatile("" :: "s"(P.x), "s"(P.lre), "s"(P.lim), "s"(P.bre), "s"(P.bim), "s"(P.cre), "s"(P.cim), "s"(P.logdt), \
                                        "s"(P.dskip), "s"(P.wqkv), "s"(P.wphi), "s"(P.wo), "s"(P.gw), "s"(P.out), "s"(P.ws))
#define WSRSRC() char* wsp = P.ws; asm volatile("" : "+s"(wsp)); const rsrc_t ws = __builtin_amdgcn_make_buffer_rsrc(wsp, 0, WS_END, 0x00020000)
    int kc = 2 * ((r4 + rq) & 3);
    unsigned vx[8];
    auto issue_ax = [&](int kk, unsigned slot) {
        dma4(P.x + kk * 128, vx, sb + hw * 8192 + slot);
        dma4(P.x + kk * 128, vx + 4, sb + hw * 8192 + slot + 4096);
    };
    auto issue_s = [&](int kk, unsigned slot) {
        dma2(P.bre + kk * 128, vx + 5, sb + slot + 20480 + hw * 4096);
        dma2(P.bim + kk * 128, vx + 5, sb + slot + 22528 + hw * 4096);
        dma4(P.x + kk * 128 - 3072, vx, sb + slot + hw * 5120);
        dma16(P.x + kk * 128 - 3072, vx[4], sb + slot + hw * 5120 + 4096);
    };
    unsigned wraw[3] = {0u, 0u, 0u};
    if (w >= 4) {
        if (r < 16) {
#pragma unroll
            for (int u = 0; u < 8; u++) { vx[u] = vr4[u & 3] + (256 * Tt + 64 * hw + 32 * (u >> 2)) * 1152; asm volatile("" : "+v"(vx[u])); }
            asm volatile("" : "+s"(kc));
            ARGS();
            issue_ax(kc, 0);
        } else {
            kc = (tt + g) % 9;
#pragma unroll
            for (int e = 0; e < 5; e++) {
                int n = 5 * hw + e;
                int t = 160 * tt + 80 * (n & 1) + 2 * (n >> 1) + 40 * (lane >> 5) + 20 * ((lane >> 3) & 1) + ((lane >> 4) & 1);
                if (t > 2047) t = 2047;
                vx[e] = (unsigned)(t * 1152 + (((lane & 7) ^ ((lane >> 4) & 3) ^ ((n & 1) << 2)) << 4) + 3072 - (e & 3) * 1024);
            }
            vx[5] = vr4[0] + (64 * g + 16 * hw) * 1152; vx[6] = vr4[1] + (64 * g + 16 * hw) * 1152; vx[7] = 0;
#pragma unroll
            for (int u = 0; u < 8; u++) asm volatile("" : "+v"(vx[u]));
            asm volatile("" : "+s"(kc));
            ARGS();
            if (g < 9) issue_s(kc, 0);
        }
        SCHED_ALL();
    } else {
        if (r < 16) {
            unsigned vo[4];
#pragma unroll
            for (int n = 0; n < 3; n++) {
                int e = 192 * w + 64 * n + lane, row = e / 10, c = e - row * 10;
                vo[n] = (row < 72 && c < 9 ? row * 36 + c * 4 : 0) + (j >> 1) * 2592;
            }
            vo[3] = (!(w & 1) || lane < 8 ? 64 * (w & 1) + lane : 71) * 16;
            asm volatile("" : "+v"(vo[0]), "+v"(vo[1]), "+v"(vo[2]), "+v"(vo[3]));
            SCHED_ALL();
#pragma unroll
            for (int n = 0; n < 3; n++) asm volatile("global_load_dword %0, %1, %2" : "+v"(wraw[n]) : "v"(vo[n]), "s"(P.wphi) : "memory");
            dma16(P.gw, vo[3], sb + A_GW + (w & 1) * 1024);
        } else {
            size_t so = lane < 24 ? (g < 9 ? g : 0) * 128 + (lane & 7) * 16 : Cc * 144 + (lane < 33 ? lane - 24 : 8) * 16;
            asm volatile("" : "+v"(so));
            SCHED_ALL();
            const size_t p0 = lane < 8 ? (size_t)P.lre : (size_t)P.lim, p1 = lane < 24 ? (size_t)P.logdt : (size_t)P.dskip;
            const size_t src = (lane < 16 ? p0 : p1) + so;
            asm volatile("s_mov_b32 m0, %1\n\ts_nop 0\n\tglobal_load_lds_dwordx4 %0, off" :: "v"(src), "s"(sb + S_PAR) : "memory");
        }
        u32x2 ep = {0u, 0u};
        asm volatile("s_cmp_eq_u32 %6, 2\n\ts_cbranch_scc0 1f\n\tglobal_load_dwordx2 %0, %4, %5 sc1\n1:\n\ts_waitcnt vmcnt(0)"
                     : "+v"(ep), "+v"(wraw[0]), "+v"(wraw[1]), "+v"(wraw[2]) : "v"(WS_EPOCH), "s"(P.ws), "s"(w) : "memory", "scc");
        if (tid == 128) {
            WSRSRC();
            const u32x2 t = __builtin_bit_cast(u32x2, __builtin_bit_cast(u64, ep) + 1);
            sts64(sb + L_TAG, t); sts128(sb + L_DONE, u32x4{0u, 0u, 0u, 0u}); st64(ws, WS_START + b * 8, 0, t);
        }
    }
#define TAGS() u32x2 tag = lds64(sb + L_TAG); tag = u32x2{(unsigned)uni(tag[0]), (unsigned)uni(tag[1])}; const unsigned tg = tag[0] & 0xbfffbfffu

    if (__builtin_expect(w >= 4 && r < 16, 1)) {
        unsigned vw[4];
        auto prep_w = [&]() {
            const unsigned oq = ((72 * jq) & ~15) + 3072, okv = ((72 * j) & ~15) + 3072;
#pragma unroll
            for (int u = 0; u < 4; u++) {
                const unsigned e = 64 * (4 * hw + u) + lane, pos = (e * 4370u) >> 16, c = e - pos * 15, part = (c * 52u) >> 8;
                const unsigned k = (pos & ~7u) | ((pos & 1) << 2) | ((pos >> 1) & 3);
                vw[u] = k * 3456 + part * 1072 + c * 16 + (part ? okv : oq) - u * 1024;
                asm volatile("" : "+v"(vw[u]));
            }
        };
        auto issue_w = [&](int kk, unsigned slot) {
            const char* wsrc = P.wqkv + (size_t)kk * (64 * 3456) - 3072;
            if (hw < 3) dma4(wsrc, vw, sb + slot + 32768 + hw * 4096); else dma3(wsrc, vw, sb + slot + 32768 + hw * 4096);
        };
        prep_w();
        issue_w(kc, 0);
        WSRSRC();
        unsigned vh = lane < 8 ? WS_KVH + (unsigned)(jq * 8 + lane) * 128u : OOR;
        unsigned vo[6];
#pragma unroll
        for (int u = 0; u < 3; u++) {
            int e = 64 * (u == 2 ? 8 : 2 * hw + u) + lane; int pos = e / 9, c = e - pos * 9;
            int ks = pos >> 5, pp = pos & 15, m = pp >> 1;
            int kq = 2 * ((pos >> 4) & 1) + (m >> 2), je = 4 * (pp & 1) + (m & 3);
            vo[u] = (unsigned)((36 * ks + 18 * (kq >> 1) + 8 * (kq & 1) + je) * 1152 + Cc * 144 + c * 16 + (u == 1 ? 0 : 1024));
            vo[3 + u] = (unsigned)((36 * (8 * ks + 2 * kq + (je >> 2)) + 18 * ((je >> 1) & 1) + 16 + (je & 1)) * 1152 + Cc * 144 + c * 16 + (u == 1 ? 0 : 1024));
        }
        const int hh = hw >> 1, dt = hw & 1;
        unsigned aa = sb + A_IMG + trow * 240 + hh * 80 + tp * 8, aa2 = sb + A_IMG + trow * 240 + hh * 80 + 64 + (tp & 1) * 8;
        unsigned ab = sb + A_IMG + trow * 240 + 160 + hh * 40 + (dt ? 32 : tp * 8);
        unsigned sa[3], da[2], v0, v1;
        {
            const unsigned e0 = dt ? 320 + hh * 40 + q4 * 4 + l15 : hh * 160 + q4 * 16 + l15, st = dt ? 16 : 64;
#pragma unroll
            for (int ft = 0; ft < 3; ft++) sa[ft] = ((!dt || l15 < 4) && (ft < 2 || q4 < 2)) ? (e0 + ft * st) * 16 + WS_KV + (unsigned)(j * 8 + Tt) * KVB : OOR;
            const int g0 = 104 * hw + lane;
            const bool two = lane < 40 && g0 + 64 < 400;
#pragma unroll
            for (int h2 = 0; h2 < 2; h2++) {
                int e = (h2 && two) ? g0 + 64 : g0, h, grp, d;
                if (e < 320) { h = e >= 160; grp = (e - 160 * h) >> 4; d = (e & 15) + 2 * h; }
                else { e -= 320; h = e >= 40; grp = (e - 40 * h) >> 2; e &= 3; d = e == 2 ? 18 : (e == 3 ? -1 : (h ? e : 16 + e)); }
                int sg = grp >= 5;
                da[h2] = (d < 0 || (h2 && !two)) ? sb + L_DUMMY : sb + A_KVS + ((h * 19 + d) * 2 + sg) * 48 + (grp - 5 * sg) * 8;
            }
            v0 = g0 * 16; v1 = two ? v0 + 1024 : OOR;
        }
        u32x4 sel = sel_frag(l15, q4);
        asm volatile("" : "+v"(aa), "+v"(aa2), "+v"(ab), "+v"(sa[0]), "+v"(sa[1]), "+v"(sa[2]), "+v"(da[0]), "+v"(da[1]), "+v"(v0), "+v"(v1), "+v"(sel), "+v"(vh));
        asm volatile("" : "+v"(vo[0]), "+v"(vo[1]), "+v"(vo[2]), "+v"(vo[3]), "+v"(vo[4]), "+v"(vo[5]));
        unsigned slot = A_STG;
#pragma unroll 1
        for (int it = 0; it < 8; it++) {
            kc++; if (kc >= 9) kc -= 9;
            issue_ax(kc, slot);
            wait_vm<8>();
            barrier();
            issue_w(kc, slot);
            slot += A_STG; if (slot == 3 * A_STG) slot = 0;
        }
        wait_vm<0>();
        auto dma_wo = [&](int u0) {
            const int u = (u0 + 2 * rq) & 7;
            dma2(P.wo + ((u + 2 * rota) & 7) * 72 * 1152 - 1024, vo, sb + u * 9216 + hw * 2048);
        };
        barrier();
#pragma unroll
        for (int u = 0; u < 4; u++) dma_wo(u);
        barrier();
#pragma unroll
        for (int u = 4; u < 8; u++) dma_wo(u);
        dma2(P.wo - 1024, vo + 3, sb + 8 * 9216 + hw * 2048);
        dma16(P.wo + ((hw + 2 * rota) & 7) * 72 * 1152 - 1024, vo[2], sb + hw * 9216 + 8192);
        dma16(P.wo + ((hw + 4 + 2 * rota) & 7) * 72 * 1152 - 1024, vo[2], sb + (hw + 4) * 9216 + 8192);
        if (hw == 0) dma16(P.wo - 1024, vo[5], sb + 8 * 9216 + 8192);
        TAGS();
        u32x4 gr[2][8];
#pragma unroll
        for (int t = 0; t < 8; t++) { gr[0][t] = gr[1][t] = u32x4{0u, 0u, 0u, 0u}; asm volatile("" : "+v"(gr[0][t]), "+v"(gr[1][t])); }
        const unsigned kb = WS_KV + (unsigned)jq * (8 * KVB);
        unsigned tg1 = v1 == OOR ? 0u : tg, hint = tid == 256 ? WS_KVH + (unsigned)(j * 8 + Tt) * 128u : OOR;
        asm volatile("" : "+v"(tg1), "+v"(hint));
        const u32x4 wsd = {(unsigned)(size_t)P.ws, (unsigned)((size_t)P.ws >> 32) & 0xffffu, WS_END, 0x00020000u};
        {
            f32x4 kv[3] = {{0, 0, 0, 0}, {0, 0, 0, 0}, {0, 0, 0, 0}};
            __builtin_amdgcn_s_setprio(2);
            barrier();
            u32x4 fa[8][3], fb[8];
            auto rdk = [&](int k) {
                fb[k] = ldstr2(ab + k * 7680, 240);
                fa[k][0] = ldstr2(aa + k * 7680, 240); fa[k][1] = ldstr2(aa + 32 + k * 7680, 240); fa[k][2] = ldstr2(aa2 + k * 7680, 240);
            };
            rdk(0); rdk(1);
#pragma unroll
            for (int k = 0; k < 8; k++) {
                SCHED_ALL();
                if (k + 2 < 8) rdk(k + 2);
                SCHED_ALL();
                if (k == 7) st32(ws, hint, 0, tg);
#pragma unroll
                for (int ft = 0; ft < 3; ft++) kv[ft] = mfma32(fa[k][ft], fb[k], kv[ft]);
            }
            SCHED_ALL();
#pragma unroll
            for (int ft = 0; ft < 3; ft++) st128(ws, sa[ft], 0, u32x4{pk(kv[ft][0], kv[ft][1]), tg, pk(kv[ft][2], kv[ft][3]), tg});
        }
        {
            unsigned todo = 0xffu;
            f32x4 s0 = {0, 0, 0, 0}, s1 = {0, 0, 0, 0};
#pragma unroll 1
            for (int spin = 0; spin < SPIN_CAP; spin++) {
                asm volatile("" ::: "memory");
                __builtin_amdgcn_s_sleep(2);
                unsigned m = (unsigned)__ballot(ld32(ws, vh, 0) == tg) & todo;
#pragma unroll
                for (int t = 0; t < 8; t++)
                    asm volatile("s_bitcmp1_b32 %2, %3\n\ts_cbranch_scc0 1f\n\tbuffer_load_dwordx4 %0, %4, %6, %7 offen sc1\n\tbuffer_load_dwordx4 %1, %5, %6, %7 offen sc1\n1:"
                                 : "+v"(gr[0][t]), "+v"(gr[1][t]) : "s"(m), "i"(t), "v"(v0), "v"(v1), "s"(wsd), "s"(kb + t * KVB) : "memory", "scc");
                todo &= ~m;
                if (!todo) {
                    asm volatile("s_waitcnt vmcnt(0)" : "+v"(gr[0][0]), "+v"(gr[0][1]), "+v"(gr[0][2]), "+v"(gr[0][3]), "+v"(gr[0][4]), "+v"(gr[0][5]), "+v"(gr[0][6]), "+v"(gr[0][7]),
                                 "+v"(gr[1][0]), "+v"(gr[1][1]), "+v"(gr[1][2]), "+v"(gr[1][3]), "+v"(gr[1][4]), "+v"(gr[1][5]), "+v"(gr[1][6]), "+v"(gr[1][7]) :: "memory");
                    unsigned x = 0;
                    s0 = s1 = f32x4{0, 0, 0, 0};
#pragma unroll
                    for (int t = 0; t < 8; t++) {
                        s0 = mfma32(sel, gr[0][t], s0); s1 = mfma32(sel, gr[1][t], s1);
                        x |= tagdiff(gr[0][t], tg) | tagdiff(gr[1][t], tg1);
                    }
                    asm volatile("" : "+v"(s0), "+v"(s1), "+v"(x));
                    if (__all(x == 0)) break;
                    todo = 0xffu;
                }
            }
            wait_vm<0>();
            sts64(da[0], pk4(s0)); sts64(da[1], pk4(s1));
        }
        __builtin_amdgcn_s_setprio(0);
        barrier();
        {
            unsigned of = lane < 16 ? WS_OFLAG + (unsigned)(Tt * 16 + l15) * 128u : OOR, om = l15 == j ? of : OOR;
            asm volatile("" : "+v"(of), "+v"(om));
            barrier();
            if (hw == 0) { st64(ws, om, 0, tag); poll_flags(ws, of, lane < 16, tag); }
            barrier();
            unsigned rd[9], gl[9];
            f32x4 ex[9];
#pragma unroll
            for (int n = 0; n < 9; n++) {
                int e = 64 * n + lane, row = e / 18, c = e - row * 18;
                rd[n] = sb + A_OUT + hw * 9728 + row * 304 + c * 16; gl[n] = row * 2304 + c * 16;
                ex[n][0] = *(LAS float*)(size_t)(sb + A_G + (128 * (R & 1) + 32 * hw + row) * 4);
            }
#pragma unroll
            for (int n = 0; n < 9; n++) asm volatile("" : "+v"(rd[n]), "+v"(gl[n]), "+v"(ex[n][0]));
            __builtin_amdgcn_s_sleep(26);
            __builtin_amdgcn_s_setprio(2);
            poll_flags<2>(ws, lane ? OOR : WS_YAF + (unsigned)(pair * 4 + hw) * 128u, lane == 0, tag);
            const unsigned yso = WS_YA + (unsigned)(pair * 4 + hw) * 5120u;
            u32x4 v[4];
#pragma unroll
            for (int t = 0; t < 4; t++) v[t] = ld128(ws, lane * 16 + t * 1024, yso);
            const u32x2 v8 = ld64(ws, lane * 8, yso + 4096);
            SCHED_ALL();
#pragma unroll
            for (int n = 0; n < 9; n++) {
                const u32x2 a = n < 8 ? u32x2{v[n >> 1][2 * (n & 1)], v[n >> 1][2 * (n & 1) + 1]} : v8;
                const float g = ex[n][0];
                ex[n][0] = bflo(a[0]) * g; PIN(ex[n][0]); ex[n][1] = bfhi(a[0]) * g; PIN(ex[n][1]);
                ex[n][2] = bflo(a[1]) * g; PIN(ex[n][2]); ex[n][3] = bfhi(a[1]) * g; PIN(ex[n][3]);
            }
            const rsrc_t ro = __builtin_amdgcn_make_buffer_rsrc((char*)P.out + (size_t)(16 * rt0) * 2304 + 288 * Cc, 0, 31 * 2304 + 288, 0x00020000);
            __builtin_amdgcn_s_setprio(0);
#pragma unroll 1
            for (int spin = 0; spin < 64 * SPIN_CAP; spin++) {
                if (uni(__hip_atomic_load((LAS unsigned*)(size_t)(sb + L_DONE + hw * 4), __ATOMIC_RELAXED, __HIP_MEMORY_SCOPE_WORKGROUP))) break;
            }
            asm volatile("" ::: "memory");
            f32x4 yv[9];
#pragma unroll
            for (int n = 0; n < 9; n++) yv[n] = __builtin_bit_cast(f32x4, lds128(rd[n]));
            SCHED_ALL();
#pragma unroll
            for (int n = 0; n < 9; n++) __builtin_amdgcn_raw_buffer_store_b128(__builtin_bit_cast(u32x4, yv[n] + ex[n]), ro, gl[n], 0, AUX_NT);
        }
    } else if (w >= 4) {
        {
            auto dma_xt = [&]() {
#pragma unroll
                for (int n = 0; n < 5; n++) {
                    int e = 64 * n + lane; if (e > 287) e = 287;
                    int row = e / 9, c = e - row * 9;
                    dma16(P.x + (size_t)(16 * rt0) * 1152 + Cc * 144, row * 1152 + c * 16, sb + S_TR + hw * 10496 + n * 1024);
                }
            };
            if (g < 9) {
                unsigned slot = S_STG;
#pragma unroll 1
                for (int it = -1; it < 7; it++) {
                    if (it >= 0) { wait_vm<9>(); barrier(); }
                    kc++; if (kc >= 9) kc -= 9;
                    issue_s(kc, slot);
                    slot += S_STG; if (slot == 3 * S_STG) slot = 0;
                }
            }
            if (g >= 9) { __builtin_amdgcn_s_sleep(80); __builtin_amdgcn_s_sleep(80); }
            if (hw < 2) dma16((hw ? P.cim : P.cre) + (size_t)((72 * Cc + 64) * 1152 + 1024), vr4[0], sb + S_PAR + 1024 + hw * 1024);
            dma_xt();
            auto dma_c2 = [&](int e) { dma_cimg(P, 2 * e + (hw >> 1), hw & 1, Cc, vr4, sb + e * S_STG + hw * 9216); };
            if (g < 9) { wait_vm<14>(); barrier(); wait_vm<5>(); barrier(); }
            wait_vm<0>();
#pragma unroll 1
            for (int e = 0; e < 2; e++) {
                if (e == 0 || g < 9) barrier();
                dma_c2(e);
            }
            const unsigned hfo = WS_HFLAG + (unsigned)(((lane & 16) ? (128 * R + 127) / 160 : (128 * R) / 160) * 9 + (l15 < 9 ? l15 : 8)) * 128u;
            WSRSRC();
            TAGS();
            barrier();
            if (hw == 3 && g < 9) st64(ws, lane ? OOR : 0u, WS_HFLAG + (unsigned)(tt * 9 + g) * 128u, tag);
#pragma unroll 1
            for (int e = 2; e < 4; e++) dma_c2(e);
            if (hw == 3) poll_flags(ws, lane < 32 ? hfo : OOR, lane < 32, tag);
            wait_vm<0>();
            asm volatile(".Lfk_end3:");
            barrier();
            __builtin_amdgcn_s_sleep(24);
            dma4(((hw >> 1) ? P.cim : P.cre) + (size_t)((72 * Cc + 32 * (hw & 1)) * 1152 + 1024), vr4, sb + 4 * S_STG + hw * 4096);
            wait_vm<0>();
            barrier();
            if (g >= 9 && sp == 0 && hw == 1) {
#pragma unroll 1
                for (int e = 0; e < 4; e++) poll_flags(ws, WS_START + (unsigned)(e * 64 + lane) * 8u, true, tag);
                if (lane == 0) st64(ws, WS_EPOCH, 0, tag);
            }
        }
    } else if (r >= 16) {
        if (g >= 9) {
            if (sp < 8) {
#pragma unroll 1
                for (int n = w; n < 9; n += 4) touch64(P.wqkv + (size_t)8 * 221184 + (n / 3) * 1152 + ((xcd >> 1) * 2 + n % 3) * 128, lane * 3456, sb + L_DUMMY);
            }
            __builtin_amdgcn_s_sleep(80);
#pragma unroll 1
            for (int n = sp * 4 + w; n < 29 + 243; n += 44) {
                int m = n - 29, a = m / 81;
                touch64(m < 0 ? P.ws + WS_HFLAG + n * 8192 : (a == 0 ? P.cre : (a == 1 ? P.cim : P.wo)) + (m - 81 * a) * 8192, lane * 128, sb + L_DUMMY);
            }
        }
        {
            WSRSRC();
            f32x4 y[2][5];
            unsigned dv[5];
#pragma unroll
            for (int t = 0; t < 5; t++) dv[t] = *(LAS u16*)(size_t)(sb + S_PAR + 384 + (16 * t + l15) * 2);
            auto prep_d = [&]() {
#pragma unroll
                for (int t = 0; t < 5; t++) {
                    bool on = q4 == 2 * (t & 1) + (l15 >> 3) && (t < 4 || l15 < 8);
                    dv[t] = on ? dv[t] << (16 * (l15 & 1)) : 0u;
                    asm volatile("" : "+v"(dv[t]));
                }
            };
            u32x4 c8[4];
            u32x2 tag;
            auto init_y = [&]() {
#pragma unroll
                for (int s = 0; s < 4; s++) c8[s] = lds128(sb + S_PAR + 1024 + (q4 >> 1) * 1024 + (l15 & 7) * 128 + ((((q4 & 1) + 2 * s) ^ ((l15 & 7) >> 1)) << 4));
                tag = lds64(sb + L_TAG);
                u32x4 xb[2][3];
#pragma unroll
                for (int m = 0; m < 2; m++)
#pragma unroll
                    for (int s = 0; s < 3; s++) xb[m][s] = lds128(sb + S_TR + w * 10496 + (16 * m + l15) * 144 + s * 64 + q4 * 16);
                const int dw = (l15 & 7) >> 1;
#pragma unroll
                for (int t = 0; t < 5; t++) {
                    u32x4 a = {dw == 0 ? dv[t] : 0u, dw == 1 ? dv[t] : 0u, dw == 2 ? dv[t] : 0u, dw == 3 ? dv[t] : 0u};
#pragma unroll
                    for (int m = 0; m < 2; m++) { y[m][t] = mfma32(a, xb[m][t >> 1], f32x4{0, 0, 0, 0}); asm volatile("" : "+v"(y[m][t])); }
                }
            };
            if (g < 9) {
                f32x2 lam, coef, l20, l40, pw, wq, l640;
                {
                    const unsigned pa = sb + S_PAR + (16 * w + l15) * 2;
                    const unsigned ra = *(LAS u16*)(size_t)pa, rb = *(LAS u16*)(size_t)(pa + 128), rd = *(LAS u16*)(size_t)(pa + 256);
                    float a = bflo(ra), bb = bflo(rb);
                    float dt = bflo(pk(expf(bflo(rd)), 0.f));
                    float e = expf(dt * a), sn, cs;
                    sincosf(dt * bb, &sn, &cs);
                    lam = f32x2{e * cs, e * sn};
                    float nr = lam[0] - 1.f, ni = lam[1], inv = 2.f / (a * a + bb * bb);
                    coef = f32x2{(nr * a + ni * bb) * inv, (ni * a - nr * bb) * inv};
                    f32x2 l2 = cmul(lam, lam), l4 = cmul(l2, l2), l5 = cmul(l4, lam), l10 = cmul(l5, l5);
                    l20 = cmul(l10, l10); l40 = cmul(l20, l20);
                    f32x2 l80 = cmul(l40, l40), l160 = cmul(l80, l80), l320 = cmul(l160, l160);
                    l640 = cmul(l320, l320);
                    const f32x2 one = {1.f, 0.f};
                    pw = cmul((q4 & 1) ? l40 : one, (q4 & 2) ? l80 : one);
                    wq = cmul((q4 & 1) ? l160 : one, (q4 & 2) ? l320 : one);
                    asm volatile("" : "+v"(lam), "+v"(coef), "+v"(l20), "+v"(l40), "+v"(pw), "+v"(wq), "+v"(l640));
                }
                prep_d();
                f32x4 acc[10][2];
#pragma unroll
                for (int i = 0; i < 10; i++) { acc[i][0] = acc[i][1] = f32x4{0, 0, 0, 0}; asm volatile("" : "+v"(acc[i][0]), "+v"(acc[i][1])); }
                unsigned ad[4];
                ad[0] = sb + (unsigned)(l15 * 128 + ((q4 ^ ((lane >> 1) & 7)) << 4)); ad[1] = ad[0] ^ 64;
                ad[2] = ad[0] + 20480 + w * 4096; ad[3] = ad[2] ^ 64;
                asm volatile("" : "+v"(ad[0]), "+v"(ad[1]), "+v"(ad[2]), "+v"(ad[3]));
                u32x4 xf[2][10], bf[2][2];
                auto rd1 = [&](int s, int n) { if (n < 2) bf[s][n] = lds128(ad[2 + s] + n * 2048); else if (n < 12) xf[s][n - 2] = lds128(ad[s] + (n - 2) * 2048); };
                int slot = 0;
                auto half = [&](int s) {
                    int ds = 0;
                    if (s == 0) ds = slot == 2 ? -2 * (int)S_STG : (int)S_STG;
                    wait_both<63>();
                    SCHED_ALL();
                    ordered<20>([&](int n) { mfma_ip(acc[n >> 1][n & 1], xf[s][n >> 1], bf[s][n & 1]); },
                                [&](int n) {
                                    rd1(s ^ 1, n);
                                    if (s == 0 && n >= 13 && n < 17) { ad[n - 13] += ds; asm volatile("" : "+v"(ad[n - 13])); }
                                    if (s == 1 && n == 14) { slot = slot == 2 ? 0 : slot + 1; asm volatile("" : "+s"(slot)); }
                                });
                    asm volatile("" :: "v"(xf[s][0]), "v"(xf[s][1]), "v"(xf[s][2]), "v"(xf[s][3]), "v"(xf[s][4]), "v"(xf[s][5]));
                };
                barrier();
                const unsigned tg = (unsigned)uni(*(LAS unsigned*)(size_t)(sb + L_TAG)) & 0xbfffbfffu;
#pragma unroll
                for (int n = 0; n < 12; n++) rd1(0, n);
                half(0);
#pragma unroll 1
                for (int it = 0; it < 8; it++) {
                    barrier();
                    half(1);
                    half(0);
                }
                mfma_drain();
                barrier();
                wait_both<63>();
                SCHED_ALL();
#pragma unroll
                for (int i = 0; i < 10; i++) { acc[i][0] = mfma32(xf[1][i], bf[1][0], acc[i][0]); acc[i][1] = mfma32(xf[1][i], bf[1][1], acc[i][1]); }
                const f32x2 lr = {lam[0], lam[0]}, li = {lam[1], lam[1]};
                f32x2 er = lo2(acc[0][0]), ei = lo2(acc[0][1]);
#pragma unroll
                for (int n = 1; n < 20; n++) {
                    f32x2 ur = (n & 1) ? hi2(acc[n >> 1][0]) : lo2(acc[n >> 1][0]), ui = (n & 1) ? hi2(acc[n >> 1][1]) : lo2(acc[n >> 1][1]);
                    f32x2 nr = pfma(lr, er, pfma(-li, ei, ur)), ni = pfma(lr, ei, pfma(li, er, ui));
                    er = nr; ei = ni;
                }
                const f32x2 e0 = {er[0], ei[0]};
                const f32x2 el = cfma(l20, e0, f32x2{er[1], ei[1]});
                float gr[4], gi[4];
                gather4(el[0], gr); gather4(el[1], gi);
                f32x2 pre = {0.f, 0.f}, run = {gr[0], gi[0]};
#pragma unroll
                for (int s = 1; s < 4; s++) { if (s == q4) pre = run; run = cfma(l40, run, f32x2{gr[s], gi[s]}); }
                st128(ws, q4 == 0 ? (w * 16 + l15) * 16 : OOR, WS_E + (unsigned)(tt * 9 + g) * 1024u,
                      u32x4{__float_as_uint(run[0]), tg, __float_as_uint(run[1]), tg});
                asm volatile("" ::: "memory");
                init_y();
#pragma unroll
                for (int i = 0; i < 10; i++) {
                    f32x4 ur = acc[i][0], ui = acc[i][1];
                    acc[i][0] = coef[0] * ur - coef[1] * ui;
                    acc[i][1] = -coef[0] * ui - coef[1] * ur;
                    asm volatile("" : "+v"(acc[i][0]), "+v"(acc[i][1]));
                }
                unsigned ea[3], et[3];
#pragma unroll
                for (int m = 0; m < 3; m++) { int t2 = tt - 1 - q4 - 4 * m; ea[m] = t2 >= 0 ? (unsigned)(t2 * 9216 + (w * 16 + l15) * 16) : OOR; et[m] = t2 >= 0 ? tg : 0u; }
                f32x2 ca0 = cmul(coef, pw), ca1 = cmul(ca0, l20), cb0 = cmul(coef, pre), cb1 = cmul(coef, cfma(l20, pre, e0));
                unsigned tw = sb + S_TR + w * 10496 + l15 * 328 + q4 * 80, ta = sb + S_TR + w * 10496 + (8 * q4 + tq) * 328 + tp * 8;
                unsigned ha = lane * 16 + WS_H + (unsigned)(10 * tt * 36 + 4 * g + w) * 1024u;
                asm volatile("" : "+v"(ca0), "+v"(ca1), "+v"(cb0), "+v"(cb1), "+v"(tw), "+v"(ta), "+v"(ha));
                u32x4 ev[3];
#pragma unroll 1
                for (int spin = 0; spin < SPIN_CAP; spin++) {
                    asm volatile("" ::: "memory");
#pragma unroll
                    for (int m = 0; m < 3; m++) ev[m] = ld128(ws, ea[m], WS_E + (unsigned)g * 1024u);
                    unsigned x = tagdiff(ev[0], et[0]) | tagdiff(ev[1], et[1]) | tagdiff(ev[2], et[2]);
                    asm volatile("" : "+v"(x));
                    if (__all(x == 0)) break;
                    __builtin_amdgcn_s_sleep(1);
                }
                asm volatile("s_barrier" ::: "memory");
                auto evc = [&](int m) { return f32x2{__uint_as_float(ev[m][0]), __uint_as_float(ev[m][2])}; };
                f32x2 cr = cmul(wq, cfma(l640, cfma(l640, evc(2), evc(1)), evc(0)));
                cr = f32x2{sum4(cr[0]), sum4(cr[1])};
                const f32x2 h0 = cfma(ca0, cr, cb0), h1 = cfma(ca1, cr, cb1);
                f32x2 hr = {h0[0], h1[0]}, hi = {-h0[1], -h1[1]};
#pragma unroll
                for (int i = 0; i < 5; i++) {
                    f32x2 vr[4], vi[4];
#pragma unroll
                    for (int k = 0; k < 4; k++) {
                        int n = 4 * i + k;
                        f32x2 ur = (n & 1) ? hi2(acc[n >> 1][0]) : lo2(acc[n >> 1][0]), ui = (n & 1) ? hi2(acc[n >> 1][1]) : lo2(acc[n >> 1][1]);
                        f32x2 nr = pfma(lr, hr, pfma(li, hi, ur)), ni = pfma(lr, hi, pfma(-li, hr, ui));
                        hr = nr; hi = ni; vr[k] = nr; vi[k] = ni;
                    }
#pragma unroll
                    for (int s = 0; s < 2; s++) {
                        sts64(tw + s * 40 + i * 8, u32x2{pk(vr[0][s], vr[1][s]), pk(vr[2][s], vr[3][s])});
                        sts64(tw + 16 * 328 + s * 40 + i * 8, u32x2{pk(vi[0][s], vi[1][s]), pk(vi[2][s], vi[3][s])});
                    }
                }
                wait_lgkm();
#pragma unroll
                for (int i = 0; i < 10; i++) {
                    u32x4 f = ldstr2(ta + i * 32, 4 * 328);
                    st128(ws, (i < 8 || tt < 12) ? ha + i * 36864 : OOR, 0, f);
                }
                wait_vm<0>();
            } else {
                prep_d();
                barrier();
                init_y();
                wait_vm<0>();
            }
            barrier();
            unsigned ca[4], vl0 = lane * 16, vl1 = vl0 + 36864, ya = vl0 + WS_YA + (unsigned)(pair * 4 + w) * 5120u;
#pragma unroll
            for (int s = 0; s < 4; s++) {
                ca[s] = sb + (unsigned)((q4 >> 1) * 9216 + l15 * 128 + ((((q4 & 1) + 2 * s) ^ ((l15 >> 1) & 7)) << 4));
                asm volatile("" : "+v"(ca[s]));
            }
            const unsigned yb = sb + S_STG + w * 4864;
            unsigned c8d = 36864 - (q4 >> 1) * 1024;
            unsigned yw = yb + l15 * 152 + q4 * 8, yw4 = yb + l15 * 152 + (q4 < 2 ? 128 + q4 * 8 : 144), yr[9];
#pragma unroll
            for (int n = 0; n < 9; n++) { int e = 64 * n + lane, row = e / 18; yr[n] = yb + row * 152 + (e - row * 18) * 8; asm volatile("" : "+v"(yr[n])); }
            asm volatile("" : "+v"(vl0), "+v"(vl1), "+v"(ya), "+v"(yw), "+v"(yw4), "+v"(c8d));
            barrier();
            {
                u32x4 af[36][2], wf[2][5];
                unsigned hs = WS_H + (unsigned)rt0 * 36864u;
                auto req = [&](int n) {
                    af[n >> 1][n & 1] = ld128(ws, ((n & 1) ? vl1 : vl0) + ((n >> 1) & 3) * 1024, hs);
                    if ((n & 7) == 7) { hs += 4096; asm volatile("" : "+s"(hs)); }
                };
#pragma unroll
                for (int n = 0; n < issued(0, 36); n++) { req(n); SCHED_ALL(); }
#pragma unroll
                for (int t = 0; t < 5; t++) wf[0][t] = lds128(ca[0] + t * 2048);
                static_for<36>([&](auto ic) {
                    constexpr int i = ic.value, n = i + 1;
                    SCHED_ALL();
                    wait_both<issued(i, 36) - 2 * i - 2>();
                    if (i == 20) asm volatile("s_barrier" ::: "memory");
                    SCHED_ALL();
                    p2_step<false>(y, wf[i & 1], af[i],
                        [&](int t) { if (n < 36) wf[n & 1][t] = (n < 32 || t < 4) ? lds128(ca[n & 3] + (n < 32 ? ((n >> 2) % 3) * 18432 : 0) + t * 2048) : c8[n & 3]; },
                        [&](int m) {
                            if (issued(i, 36) + m < issued(n, 36)) req(issued(i, 36) + m);
                            if (m == 2 && ((n >> 2) == 2 || (n >> 2) == 5 || (n >> 2) == 7)) { ca[n & 3] += (n >> 2) < 7 ? 55296 : c8d; asm volatile("" : "+v"(ca[n & 3])); } });
                });
                mfma_drain();
            }
#pragma unroll
            for (int m = 0; m < 2; m++)
#pragma unroll
                for (int t = 0; t < 5; t++) sts64((t < 4 ? yw + t * 32 : yw4) + m * 2432, pk4(y[m][t]));
            wait_lgkm();
            u32x2 pc8[9];
#pragma unroll
            for (int n = 0; n < 9; n++) pc8[n] = lds64(yr[n]);
            SCHED_ALL();
#pragma unroll
            for (int t = 0; t < 4; t++) st128(ws, ya + t * 1024, 0, u32x4{pc8[2 * t][0], pc8[2 * t][1], pc8[2 * t + 1][0], pc8[2 * t + 1][1]});
            st64(ws, ya - lane * 8, 4096, pc8[8]);
            wait_vm<0>();
            st64(ws, lane ? OOR : 0u, WS_YAF + (unsigned)(pair * 4 + w) * 128u, tag);
            asm volatile(".Lfk_end2:");
        }
    } else {
        WSRSRC();
        if (tid < 232) sts128(sb + A_KVS + tid * 16, u32x4{0u, 0u, 0u, 0u});
#pragma unroll
        for (int n = 0; n < 3; n++) {
            int e = 192 * w + 64 * n + lane, row = e / 10, c = e - row * 10;
            unsigned v = c < 9 ? wraw[n] : 0u;
            if (row < 72) { *(LAS unsigned*)(size_t)(sb + A_WP + e * 4) = v; *(LAS unsigned*)(size_t)(sb + A_WP + 2880 + e * 4) = c < 9 ? v ^ 0x80008000u : 0u; }
            else *(LAS unsigned*)(size_t)(sb + A_WP + 2880 + e * 4) = e == 739 ? 0x3f80u : 0u;
        }
        auto waddr = [&](int side, unsigned (&a)[3]) {
            const unsigned zr = sb + A_WP + 5760, base = sb + A_WP + (tp & 1) * 2880 + ((side ? j : jq) & 1) * 1440;
            const int c0 = 4 * q4 + tq, c1 = 16 + c0;
            a[0] = tp < 2 ? base + c0 * 40 : zr; a[1] = (c1 >= 18) == (tp >= 2) ? base + c1 * 40 : zr;
            a[2] = (q4 == side && tp >= 2) ? base + (32 + tq) * 40 : ((!side && q4 == 3 && tq == 0) ? zr + 40 : zr);
        };
        auto wfrag = [&](const unsigned (&a)[3], u32x4 (&am)[5], u32x4 (&al)[5]) {
#pragma unroll
            for (int u = 0; u < 5; u++) {
                u32x2 lo = ldstr(a[0] + u * 8), hi = ldstr(a[1] + u * 8);
                am[u] = u32x4{lo[0], lo[1], hi[0], hi[1]}; al[u] = pad4(ldstr(a[2] + u * 8));
            }
        };
        unsigned wa[3];
        waddr(1, wa);
        f32x4 acc[7][4];
#pragma unroll
        for (int i = 0; i < 7; i++)
#pragma unroll
            for (int c = 0; c < 4; c++) { acc[i][c] = f32x4{0, 0, 0, 0}; asm volatile("" : "+v"(acc[i][c])); }
        unsigned ad[6];
        ad[0] = sb + (unsigned)((64 * w + l15) * 128 + ((q4 ^ ((lane >> 1) & 7)) << 4)); ad[1] = ad[0] ^ 64;
        ad[2] = sb + 32768 + trow * 240 + (jq & 1) * 8 + tp * 8;
        ad[3] = sb + 32768 + trow * 240 + (j & 1) * 8 + tp * 8;
        ad[4] = sb + 32768 + trow * 240 + ((tp ? j : jq) & 1) * 8 + 64 + 80 * (tp > 2 ? 2 : tp);
        ad[5] = sb + A_GW + q4 * 16 + kc * 128;
        unsigned irow = sb + A_IMG + (unsigned)((64 * w + l15) * 240);
        unsigned ia = irow + q4 * 40, iv = irow + 160 + q4 * 8, il = irow + (q4 == 0 ? 232 : (q4 == 2 ? 224 : q4 * 40));
        asm volatile("" : "+v"(ia), "+v"(iv), "+v"(il), "+v"(wa[0]), "+v"(wa[1]), "+v"(wa[2]));
        asm volatile("" : "+v"(ad[0]), "+v"(ad[1]), "+v"(ad[2]), "+v"(ad[3]), "+v"(ad[4]), "+v"(ad[5]));
        u32x4 xf[2][4], gf[2];
        u32x2 wh[2][14];
        auto wf = [&](int s, int i) { return u32x4{wh[s][2 * i][0], wh[s][2 * i][1], wh[s][2 * i + 1][0], wh[s][2 * i + 1][1]}; };
        auto rd1 = [&](int s, int n) {
            if (n < 4) xf[s][n] = lds128(ad[s] + n * 2048);
            else if (n < 18) { const int i = (n - 4) >> 1; wh[s][n - 4] = ldstr((i < 2 ? ad[2] : (i < 6 ? ad[3] : ad[4])) + s * 7680 + (i < 6 ? (i >> 1) * 80 + (i & 1) * 32 : 0) + (n & 1) * 240); }
            else if (n == 18) gf[s] = lds128(ad[5] + s * 64);
        };
        auto gate = [&](int s, int h) { wh[s][12 + h] = u32x2{l15 >= 12 ? gf[s][2 * h] : wh[s][12 + h][0], l15 >= 12 ? gf[s][2 * h + 1] : wh[s][12 + h][1]}; };
        int slot = 0;
        auto half = [&](int s) {
            int ds = 0, dg = 0;
            if (s == 0) { ds = slot == 2 ? -2 * (int)A_STG : (int)A_STG; dg = kc == 8 ? -1024 : 128; }
            wait_both<63>();
            SCHED_ALL();
            ordered<28>([&](int n) { mfma_ip(acc[n >> 2][n & 3], wf(s, n >> 2), xf[s][n & 3]); },
                        [&](int n) {
                            rd1(s ^ 1, n);
                            if (n == 19 || n == 20) gate(s, n - 19);
                            if (s == 0 && n >= 21 && n < 27) { ad[n - 21] += n < 26 ? ds : dg; asm volatile("" : "+v"(ad[n - 21])); }
                            if (s == 1 && n == 22) { slot = slot == 2 ? 0 : slot + 1; kc = kc == 8 ? 0 : kc + 1; asm volatile("" : "+s"(slot), "+s"(kc)); }
                        });
            asm volatile("" :: "v"(wf(s, 0)), "v"(wf(s, 1)), "v"(wf(s, 2)), "v"(wf(s, 3)), "v"(wf(s, 4)), "v"(wf(s, 5)));
        };
        barrier();
#pragma unroll
        for (int n = 0; n < 19; n++) rd1(0, n);
        half(0);
#pragma unroll 1
        for (int it = 0; it < 8; it++) {
            barrier();
            half(1);
            half(0);
        }
        mfma_drain();
        barrier();
        wait_both<63>();
        SCHED_ALL();
        u32x4 am[5], al[5], bl[4];
        unsigned ph[4][9];
        {
            auto last = [&](int i, int c) { acc[i][c] = mfma32(wf(1, i), xf[1][c], acc[i][c]); };
            auto last5 = [&](int n, int c) { last(n == 0 ? 2 : (n == 1 ? 3 : (n == 2 ? 6 : n + 1)), c); };
            u32x4 bk[4];
            auto cvt = [&](int n, int c) {
                const f32x4 v = acc[n < 2 ? 2 : (n < 4 ? 3 : (n < 6 ? 6 : 4))][c];
                const unsigned p = (n & 1) ? pk(v[2], v[3]) : pk(v[0], v[1]);
                if (n < 4) bk[c][n] = p; else bl[c][n - 4] = p;
            };
            auto put = [&](int c, int m) { sts64(ia + c * 3840 + m * 8, u32x2{ph[c][2 * m], m < 4 ? ph[c][2 * m + 1] : 0u}); };
            gate(1, 0); gate(1, 1);
            wfrag(wa, am, al);
#pragma unroll
            for (int n = 0; n < 5; n++) last5(n, 0);
#pragma unroll
            for (int n = 0; n < 5; n++) { __builtin_amdgcn_sched_group_barrier(0x8, 1, 0); __builtin_amdgcn_sched_group_barrier(0x100, 3, 0); __builtin_amdgcn_sched_group_barrier(0x2, 2, 0); }
            SCHED_ALL();
            f32x4 t[4][5];
#pragma unroll
            for (int n = 0; n < 5; n++) { last5(n, 1); SCHED_ALL(); if (n) { cvt(2 * n - 2, 0); cvt(2 * n - 1, 0); } SCHED_ALL(); }
            wait_both<63>();
#pragma unroll
            for (int n = 0; n < 10; n++) feat_mm1(n, bk[0], bl[0], am, al, t[0]);
#pragma unroll
            for (int c = 0; c < 4; c++) {
                SCHED_ALL();
                u32x2 v5;
                feat_sm(t[c], ph[c],
                    [&](int n) { if (c < 3 && n < 8) cvt(n, c + 1); if (c < 2 && !(n & 1)) { SCHED_ALL(); last5(n >> 1, c + 2); } },
                    [&](int n) { if (c < 3) feat_mm1(n == 9 ? 8 : (n == 8 ? 9 : n), bk[c + 1], bl[c + 1], am, al, t[c + 1]);
                                 if (c && n < 9 && !(n & 1)) put(c - 1, n >> 1); },
                    [&](int n) {
                        if (n == 0) v5 = pk4(acc[5][c]);
                        if (n == 1) sts64(iv + c * 3840, u32x2{bl[c][2], bl[c][3]});
                        if (n == 2) sts64(iv + c * 3840 + 32, u32x2{v5[0], q4 ? v5[1] : 0x3f80u});
                        if (n == 3) sts64(il + c * 3840, u32x2{q4 ? bl[c][0] : v5[1], q4 ? bl[c][1] : 0x3f80u}); });
#pragma unroll
                for (int f = 0; f < 9; f++) asm volatile("" :: "v"(ph[c][f]));
            }
#pragma unroll
            for (int m = 0; m < 5; m++) put(3, m);
            SCHED_ALL();
        }
        barrier();
#pragma unroll
        for (int n = 0; n < 8; n++) acc[n & 1][n >> 1] = mfma32(wf(1, n & 1), xf[1][n >> 1], acc[n & 1][n >> 1]);
        float g1[4];
        waddr(0, wa);
        wfrag(wa, am, al);
        u32x4 p0[4], p1[4], p2[4];
        {
            f32x4 t[4][5];
#pragma unroll
            for (int n = 0; n < 10; n++) feat_mm1(n, pk8(acc[0][0], acc[1][0]), bl[0], am, al, t[0]);
#pragma unroll
            for (int c = 0; c < 4; c++) {
                SCHED_ALL();
                u32x4 bq;
                feat_sm(t[c], ph[c],
                    [&](int n) { if (c < 3 && n < 4) bq[n] = (n & 1) ? pk(acc[n >> 1][c + 1][2], acc[n >> 1][c + 1][3]) : pk(acc[n >> 1][c + 1][0], acc[n >> 1][c + 1][1]); },
                    [&](int n) { if (c < 3) feat_mm1(n == 9 ? 8 : (n == 8 ? 9 : n), bq, bl[c + 1], am, al, t[c + 1]); },
                    [&](int n) {
                        if (n == 0) g1[c] = __builtin_amdgcn_exp2f(t[c][4][2] * 1.44269504088896341f);
                        if (n == 2) g1[c] = __builtin_amdgcn_rcpf(1.f + g1[c]); });
                *(LAS float*)(size_t)(sb + A_G + (64 * w + 16 * c + l15) * 4) = 1.f - g1[c];
                p0[c] = u32x4{ph[c][0], ph[c][1], ph[c][2], ph[c][3]}; p1[c] = u32x4{ph[c][4], ph[c][5], ph[c][6], ph[c][7]}; p2[c] = u32x4{ph[c][8], 0u, 0u, 0u};
                SCHED_ALL();
                asm volatile("" : "+v"(p0[c]), "+v"(p1[c]), "+v"(p2[c]));
            }
        }
        unsigned ka[3], oa, ola;
        {
            const int oq = l15 >> 2, hh = oq >> 1, sl = l15 & 3;
            const bool on = (q4 >> 1) == hh;
#pragma unroll
            for (int u = 0; u < 3; u++) {
                int d = u < 2 ? 8 * (oq & 1) + 4 * u + sl : (sl < 3 ? 16 + sl : 18);
                ka[u] = sb + A_KVS + (on ? (unsigned)(((hh * 19 + d) * 2 + (q4 & 1)) * 48) : 3648u);
            }
            oa = lane * 16 + WS_O + (unsigned)((16 * Tt + 4 * w) * 16 + jq) * 1024u;
            ola = q4 ? OOR : l15 * 16 + WS_OL + (unsigned)((8 * Tt + 2 * w) * 16 + jq) * 256u;
            asm volatile("" : "+v"(ka[0]), "+v"(ka[1]), "+v"(ka[2]), "+v"(oa), "+v"(ola));
        }
        barrier();
        {
            u32x4 ak[3][3];
#pragma unroll
            for (int k = 0; k < 3; k++)
#pragma unroll
                for (int u = 2; u >= 0; u--) ak[k][u] = lds128(ka[u] + k * 16);
            SCHED_ALL();
            f32x4 nm[4][3];
            float inv;
            u32x4 ov, ol;
            auto fin = [&](int n, int c) {
                if (n == 0) inv = nm[c][2][2] + 1e-6f;
                if (n == 1) inv = __builtin_amdgcn_rcpf(inv);
                if (n == 2) inv *= g1[c];
                if (n < 3) PIN(inv);
                if (n >= 3 && n < 8) { const int u = (n - 3) >> 1, h = 2 * ((n - 3) & 1);
                    nm[c][u][h] *= inv; PIN(nm[c][u][h]); nm[c][u][h + 1] *= inv; PIN(nm[c][u][h + 1]); }
                if (n >= 4 && n < 8) { const int u = (n - 4) >> 1, h = 2 * ((n - 4) & 1); ov[n - 4] = pk(nm[c][u][h], nm[c][u][h + 1]); PIN(ov[n - 4]); }
                if (n == 8) {
                    st128(ws, oa, c * 16384, ov);
                    unsigned lo = pk(nm[c][2][0], nm[c][2][1]);
                    auto sw = __builtin_amdgcn_permlane32_swap(lo, lo, false, false);
                    ol[2 * (c & 1)] = sw[0]; ol[2 * (c & 1) + 1] = sw[1];
                    if (c & 1) st128(ws, ola, (c >> 1) * 4096, ol);
                }
            };
#pragma unroll
            for (int c = 0; c < 5; c++)
#pragma unroll
                for (int n = 0; n < 9; n++) {
                    const int k = n / 3, u = 2 - n % 3;
                    if (c < 4) nm[c][u] = mfma32(ak[k][u], k == 0 ? p0[c] : (k == 1 ? p1[c] : p2[c]), k ? nm[c][u] : f32x4{0, 0, 0, 0});
                    SCHED_ALL();
                    if (c) fin(n, c - 1);
                    SCHED_ALL();
                }
        }
        asm volatile("" ::: "memory");
        SCHED_ALL();
        f32x4 y[2][5];
        u32x4 af[18][2], wv[2][5];
        unsigned ow = sb + A_OUT + w * 9728 + l15 * 304 + q4 * 16, ow4 = ow + (q4 < 2 ? 256 : 288 - q4 * 16);
        unsigned vl0 = lane * 16, vl1 = vl0 + 16384, vll = q4 * 512 + l15 * 16;
        unsigned os[5], a[2] = {sb + trow * 144 + tp * 8, sb + trow * 144 + tp * 8 + 9 * 4608};
#pragma unroll
        for (int k = 0; k < 5; k++) { os[k] = k < 4 ? WS_O + (unsigned)rt0 * 16384u + ((k + rota) & 3) * 4096 : WS_OL + (unsigned)rt0 * 2048u; asm volatile("" : "+s"(os[k])); }
        asm volatile("" : "+v"(ow), "+v"(ow4), "+v"(vl0), "+v"(vl1), "+v"(vll), "+v"(a[0]), "+v"(a[1]));
#pragma unroll
        for (int t = 0; t < 5; t++) wv[0][t] = ldstr2(a[0] + t * 32, 144);
        wait_vm<0>();
        barrier();
        barrier();
        {
            auto req = [&](int n) {
                const int k = n >> 1, m = n & 1;
                if (k < 16) af[k][m] = ld128(ws, (m ? vl1 : vl0) + (k & 3) * 1024, os[k >> 2]);
                else if (k < 18) af[k][m] = ld128(ws, vll + (k - 16) * 2048 + m * 256, os[4]);
            };
#pragma unroll
            for (int n = 0; n < issued(0, 18); n++) { req(n); SCHED_ALL(); if ((n & 1) && n < 8) __builtin_amdgcn_s_sleep(1); SCHED_ALL(); }
            static_for<18>([&](auto ic) {
                constexpr int i = ic.value;
                SCHED_ALL();
                wait_both<issued(i, 18) - 2 * i - 2>();
                SCHED_ALL();
                p2_step<i == 0>(y, wv[i & 1], af[i],
                    [&](int t) { if constexpr (i < 17) wv[(i + 1) & 1][t] = ldstr2(a[(i + 1) / 9] + ((i + 1) % 9) * 4608 + t * 32, 144); },
                    [&](int m) {
                        if (issued(i, 18) + m < issued(i + 1, 18)) req(issued(i, 18) + m);
                        if constexpr (i == 15 || i == 16) if (m == 2) {
                            const u32x4 p = af[i + 1][0], q = af[i + 1][1];
                            af[i + 1][0] = u32x4{p[0], p[1], q[0], q[1]}; af[i + 1][1] = u32x4{p[2], p[3], q[2], q[3]};
                            asm volatile("" : "+v"(af[i + 1][0]), "+v"(af[i + 1][1]));
                        } });
            });
            mfma_drain();
        }
#pragma unroll
        for (int m = 0; m < 2; m++)
#pragma unroll
            for (int t = 0; t < 5; t++) sts128((t < 4 ? ow + t * 64 : ow4) + m * 4864, __builtin_bit_cast(u32x4, y[m][t]));
        asm volatile(".Lfk_end:");
        asm volatile("" ::: "memory");
        __hip_atomic_store((LAS unsigned*)(size_t)(sb + L_DONE + w * 4), 1u, __ATOMIC_RELAXED, __HIP_MEMORY_SCOPE_WORKGROUP);
    }
}

extern "C" void kernel_launch(void* const* d_in, const int* in_sizes, int n_in,
                              void* d_out, int out_size, void* d_ws, size_t ws_size,
                              hipStream_t stream) {
    Params P;
    P.x = (const char*)d_in[0];
    P.lre = (const char*)d_in[1];
    P.lim = (const char*)d_in[2];
    P.bre = (const char*)d_in[3];
    P.bim = (const char*)d_in[4];
    P.cre = (const char*)d_in[5];
    P.cim = (const char*)d_in[6];
    P.logdt = (const char*)d_in[7];
    P.dskip = (const char*)d_in[8];
    P.wqkv = (const char*)d_in[10];
    P.wphi = (const char*)d_in[11];
    P.wo = (const char*)d_in[12];
    P.gw = (const char*)d_in[13];
    P.out = (float*)d_out;
    P.ws = (char*)d_ws;
    fused_kernel<<<256, 512, 0, stream>>>(P);
}
```
